```python
import torch

def _softmax(x, axis=-1):
    return torch.softmax(x, dim=axis)
def _gelu(x):
    return torch.nn.functional.gelu(x, approximate="tanh")
def _silu(x):
    return torch.nn.functional.silu(x)
def _sigmoid(x):
    return torch.sigmoid(x)
def _layernorm(x, axis=-1, eps=1e-5):
    m = x.mean(dim=axis, keepdim=True)
    v = x.var(dim=axis, keepdim=True, unbiased=False)
    return (x - m) / torch.sqrt(v + eps)
def _scatter_add(tgt, idx, src, axis=0):
    return tgt.clone().index_add_(axis, idx, src)
def _segment_sum(src, seg, n):
    out = torch.zeros((n,) + src.shape[1:], dtype=src.dtype)
    return out.index_add_(0, seg, src)
def _pad(x, pw):
    flat = tuple(v for pair in reversed(pw) for v in pair)
    return torch.nn.functional.pad(x, flat)

kernel_name = 'recurrent_scan__grouped_kv_mqa__geglu_ffn__prompt'

def setup_inputs():

    _w = (lambda a: (lambda t: t.to(torch.bfloat16) if t.is_floating_point() else t)(torch.as_tensor(a)))
    def _A_setup():
        B, S, D, H = 1, 2048, 512, 32
        Dh = D // H
        return {
            "x": _w((lambda *s: torch.randn(*s, dtype=torch.bfloat16) * 0.05)(B, S, D)),
            "wq": _w((lambda *s: torch.randn(*s, dtype=torch.bfloat16) * 0.05)(D, H * Dh)),
            "wk": _w((lambda *s: torch.randn(*s, dtype=torch.bfloat16) * 0.05)(D, Dh)),
            "wv": _w((lambda *s: torch.randn(*s, dtype=torch.bfloat16) * 0.05)(D, Dh)),
            "wo": _w((lambda *s: torch.randn(*s, dtype=torch.bfloat16) * 0.05)(H * Dh, D)),
        }
    _A_in = _A_setup()

    def _B_setup():
        B, S, D, F = 1, 2048, 512, 1792
        return {
            "x": _w((lambda *s: torch.randn(*s, dtype=torch.bfloat16) * 0.05)(B, S, D)),
            "w_gate": _w((lambda *s: torch.randn(*s, dtype=torch.bfloat16) * 0.05)(D, F)),
            "w_up": _w((lambda *s: torch.randn(*s, dtype=torch.bfloat16) * 0.05)(D, F)),
            "w_down": _w((lambda *s: torch.randn(*s, dtype=torch.bfloat16) * 0.05)(F, D)),
        }
    _B_in = _B_setup()

    return {
        "A_x": _A_in["x"],
        "A_wq": _A_in["wq"],
        "A_wk": _A_in["wk"],
        "A_wv": _A_in["wv"],
        "A_wo": _A_in["wo"],
        "B_x": _B_in["x"],
        "B_w_gate": _B_in["w_gate"],
        "B_w_up": _B_in["w_up"],
        "B_w_down": _B_in["w_down"],
    }

def reference(A_x, A_wq, A_wk, A_wv, A_wo, B_x, B_w_gate, B_w_up, B_w_down):
    def _A_ref(x, wq, wk, wv, wo):
        B, S, D = x.shape
        H = 32
        Dh = D // H
        q = (lambda x, *d: x.permute(*d))((x @ wq).reshape(B, S, H, Dh), 0, 2, 1, 3)
        k = x @ wk
        v = x @ wv
        scores = torch.einsum("bhqd,bkd->bhqk", q, k) * (Dh ** -0.5)
        p = _softmax(scores, axis=-1)
        o = torch.einsum("bhqk,bkd->bhqd", p, v)
        return (lambda x, *d: x.permute(*d))(o, 0, 2, 1, 3).reshape(B, S, H * Dh) @ wo

    def _B_ref(x, w_gate, w_up, w_down):
        return (_gelu(x @ w_gate) * (x @ w_up)) @ w_down

    x = A_x
    xn = _layernorm(x, axis=-1)
    s = _A_ref(xn, A_wq, A_wk, A_wv, A_wo)
    g = _silu(_B_ref(xn, B_w_gate, B_w_up, B_w_down))
    return x + s * g
```

```cpp
#include <hip/hip_runtime.h>
#include <hip/hip_bf16.h>
#include <stdint.h>

typedef unsigned short u16;
typedef __attribute__((ext_vector_type(8))) short bf16x8;
typedef __attribute__((ext_vector_type(4))) short s16x4;
typedef __attribute__((ext_vector_type(16))) float f32x16;
typedef __attribute__((ext_vector_type(4))) float f32x4;
typedef __attribute__((ext_vector_type(4))) unsigned u32x4;
typedef __attribute__((ext_vector_type(2))) unsigned u32x2;
#define LAS __attribute__((address_space(3)))
#define GAS __attribute__((address_space(1)))
typedef LAS const char* lds_cptr;

constexpr int S_ = 2048, D_ = 512, F_ = 1792;
constexpr size_t WS_XNF = 0;
constexpr size_t WS_KV = 2u << 20;
constexpr size_t WS_KN2 = WS_KV + (128u << 10);
constexpr size_t WS_FLAG = WS_KN2 + 4096;
constexpr size_t WS_HF = 3u << 20;
constexpr size_t WS_OF = 11u << 20;
constexpr size_t WS_DP = 13u << 20;

#define MFMA32(a, b, c) __builtin_amdgcn_mfma_f32_32x32x16_bf16(a, b, c, 0, 0, 0)

__device__ __forceinline__ unsigned cvtpk(float lo, float hi) {
  typedef __attribute__((ext_vector_type(2))) float f32x2_t;
  typedef __attribute__((ext_vector_type(2))) __bf16 bf16x2_t;
  f32x2_t v = {lo, hi};
  bf16x2_t r = __builtin_convertvector(v, bf16x2_t);
  return __builtin_bit_cast(unsigned, r);
}
__device__ __forceinline__ float bf_lo(unsigned u) { return __uint_as_float(u << 16); }
__device__ __forceinline__ float bf_hi(unsigned u) { return __uint_as_float(u & 0xffff0000u); }

__device__ __forceinline__ void glds16(const void* sbase, unsigned voff, unsigned lds_dst) {
  unsigned sv;
  asm volatile("s_mov_b32 %0, m0\n\ts_mov_b32 m0, %3\n\ts_nop 0\n\tglobal_load_lds_dwordx4 %1, %2\n\ts_mov_b32 m0, %0"
               : "=&s"(sv) : "v"(voff), "s"(sbase), "s"(lds_dst) : "memory");
}
#define WAITV(n) asm volatile("s_waitcnt vmcnt(" #n ")" ::: "memory")
#define WAIT_BAR(n) asm volatile("s_waitcnt vmcnt(" #n ") lgkmcnt(0)\n\ts_barrier" ::: "memory")
#define BAR() asm volatile("s_waitcnt lgkmcnt(0)\n\ts_barrier" ::: "memory")

__device__ __forceinline__ s16x4 trrd(lds_cptr p) {
  typedef short v4i16_t __attribute__((ext_vector_type(4)));
  return __builtin_bit_cast(s16x4, __builtin_amdgcn_ds_read_tr16_b64_v4i16((LAS v4i16_t*)p));
}
__device__ __forceinline__ bf16x8 trfrag(lds_cptr p, int second_off) {
  s16x4 a = trrd(p), b = trrd(p + second_off);
  return (bf16x8){a[0], a[1], a[2], a[3], b[0], b[1], b[2], b[3]};
}
__device__ __forceinline__ bf16x8 ldsfrag(lds_cptr p) { return *(const LAS bf16x8*)p; }

template <int CTRL> __device__ __forceinline__ float dppf(float v) {
  return __int_as_float(__builtin_amdgcn_update_dpp(0, __float_as_int(v), CTRL, 0xf, 0xf, true));
}
__device__ __forceinline__ float swap32sum(float v) {
  auto r = __builtin_amdgcn_permlane32_swap(__float_as_uint(v), __float_as_uint(v), false, false);
  return __uint_as_float(r[0]) + __uint_as_float(r[1]);
}
__device__ __forceinline__ float wave_sum(float v) {
  v += dppf<0xB1>(v);
  v += dppf<0x4E>(v);
  v += dppf<0x141>(v);
  v += dppf<0x140>(v);
  auto r = __builtin_amdgcn_permlane16_swap(__float_as_uint(v), __float_as_uint(v), false, false);
  v = __uint_as_float(r[0]) + __uint_as_float(r[1]);
  return swap32sum(v);
}
__device__ __forceinline__ float wave_max(float v) {
  v = fmaxf(v, dppf<0xB1>(v));
  v = fmaxf(v, dppf<0x4E>(v));
  v = fmaxf(v, dppf<0x141>(v));
  v = fmaxf(v, dppf<0x140>(v));
  auto r = __builtin_amdgcn_permlane16_swap(__float_as_uint(v), __float_as_uint(v), false, false);
  v = fmaxf(__uint_as_float(r[0]), __uint_as_float(r[1]));
  auto r2 = __builtin_amdgcn_permlane32_swap(__float_as_uint(v), __float_as_uint(v), false, false);
  return fmaxf(__uint_as_float(r2[0]), __uint_as_float(r2[1]));
}

constexpr int K0_XS = 0, K0_WK = 8 * 1040, K0_WV = K0_WK + 16384 + 128, K0_RED = K0_WV + 16384, K0_KVS = K0_RED + 4096, K0_LDS = K0_KVS + 512;
__global__ __launch_bounds__(256) void k0_prep(const u16* __restrict__ x, const u16* __restrict__ wk, const u16* __restrict__ wv, char* __restrict__ ws) {
  __shared__ __attribute__((aligned(16))) char lds[K0_LDS];
  const int tid = threadIdx.x, lane = tid & 63, w = __builtin_amdgcn_readfirstlane(tid >> 6), g = blockIdx.x;
  const unsigned lb = (unsigned)(uintptr_t)lds;
#pragma unroll
  for (int i = 0; i < 4; ++i) {
    glds16((const char*)wk + (w * 4 + i) * 1024, lane * 16, lb + K0_WK + (w * 4 + i) * 1024);
    glds16((const char*)wv + (w * 4 + i) * 1024, lane * 16, lb + K0_WV + (w * 4 + i) * 1024);
  }
  u32x4 xr[2];
#pragma unroll
  for (int r = 0; r < 2; ++r) xr[r] = *(const u32x4*)(x + (size_t)(8 * g + 2 * w + r) * D_ + lane * 8);
  if (tid == 0 && g < 64) *(unsigned*)(ws + WS_FLAG + g * 256) = 0u;
#pragma unroll
  for (int r = 0; r < 2; ++r) {
    float v[8];
#pragma unroll
    for (int i = 0; i < 4; ++i) { v[2 * i] = bf_lo(xr[r][i]); v[2 * i + 1] = bf_hi(xr[r][i]); }
    float s = ((v[0] + v[1]) + (v[2] + v[3])) + ((v[4] + v[5]) + (v[6] + v[7]));
    const float mean = wave_sum(s) * (1.0f / D_);
    float q = 0.f;
#pragma unroll
    for (int i = 0; i < 8; ++i) { v[i] -= mean; q += v[i] * v[i]; }
    const float var = wave_sum(q) * (1.0f / D_);
    const float rs = 1.0f / sqrtf(var + 1e-5f);
    u32x4 o;
#pragma unroll
    for (int i = 0; i < 4; ++i) o[i] = cvtpk(v[2 * i] * rs, v[2 * i + 1] * rs);
    *(LAS u32x4*)((LAS char*)lds + K0_XS + (2 * w + r) * 1040 + lane * 16) = o;
  }
  WAIT_BAR(0);
  const int tt = g >> 2, a = g & 3, t0 = a * 8;
#pragma unroll
  for (int i = 0; i < 2; ++i) {
    const int p = (2 * w + i) * 8 + (lane >> 3), ks = p >> 1, hh = p & 1, tok = lane & 7;
    u32x4 v = *(const LAS u32x4*)((lds_cptr)lds + K0_XS + tok * 1040 + ks * 32 + hh * 16);
    *(u32x4*)(ws + WS_XNF + ((size_t)(tt * 32 + ks) * 64 + hh * 32 + t0 + tok) * 16) = v;
  }
  {
    const int gI = lane >> 4, hh = gI >> 1, q = (lane & 15) >> 2, p = lane & 3;
    lds_cptr ap = (lds_cptr)lds + ((gI & 1) ? K0_WV : K0_WK) + (8 * hh + q) * 32 + p * 8 + w * 8 * 512;
    lds_cptr bp = (lds_cptr)lds + K0_XS + (lane & 7) * 1040 + hh * 16 + w * 8 * 32;
    f32x16 acc = {};
#pragma unroll
    for (int ks = 0; ks < 8; ++ks) acc = MFMA32(trfrag(ap + ks * 512, 128), ldsfrag(bp + ks * 32), acc);
    if ((lane & 31) < 8) {
#pragma unroll
      for (int r = 0; r < 16; ++r) *(LAS float*)((LAS char*)lds + K0_RED + (((w * 16 + r) * 2 + hh) * 8 + (lane & 7)) * 4) = acc[r];
    }
  }
  __syncthreads();
  {
    const int n = tid >> 3, tok = tid & 7, hh = (n >> 2) & 1, r = (n & 3) + 4 * (n >> 3);
    float s = 0.f;
#pragma unroll
    for (int ww = 0; ww < 4; ++ww) s += *(const LAS float*)((lds_cptr)lds + K0_RED + (((ww * 16 + r) * 2 + hh) * 8 + tok) * 4);
    *(LAS u16*)((LAS char*)lds + K0_KVS + (tok * 32 + n) * 2) = (u16)(cvtpk(s, 0.f) & 0xffffu);
  }
  __syncthreads();
  const LAS u16* kvs = (const LAS u16*)((lds_cptr)lds + K0_KVS);
  char* kvimg = ws + WS_KV + (size_t)(tt >> 3) * 16384;
  const int tau = tt & 7;
  if (tid < 16) {
    const int tok = tid & 7, hh = tid >> 3;
    u32x4 v;
#pragma unroll
    for (int i = 0; i < 4; ++i) {
      const int d0 = 8 * ((2 * i) >> 2) + 4 * hh + ((2 * i) & 3);
      v[i] = (unsigned)kvs[tok * 32 + d0] | ((unsigned)kvs[tok * 32 + d0 + 1] << 16);
    }
    *(u32x4*)(kvimg + tau * 1024 + (hh * 32 + t0 + tok) * 16) = v;
  } else if (tid >= 64 && tid < 96) {
    const int d = tid & 15, hh = (tid >> 4) & 1;
    u32x2 v;
    v[0] = (unsigned)kvs[(4 * hh + 0) * 32 + 16 + d] | ((unsigned)kvs[(4 * hh + 1) * 32 + 16 + d] << 16);
    v[1] = (unsigned)kvs[(4 * hh + 2) * 32 + 16 + d] | ((unsigned)kvs[(4 * hh + 3) * 32 + 16 + d] << 16);
    *(u32x2*)(kvimg + 8192 + (tau * 2 + (a >> 1)) * 512 + (hh * 16 + d) * 16 + (a & 1) * 8) = v;
  } else if (tid >= 128 && tid < 192) {
    float s = 0.f;
    const int tok = tid & 7;
#pragma unroll
    for (int d = 0; d < 16; ++d) { const float f = __uint_as_float((unsigned)kvs[tok * 32 + d] << 16); s += f * f; }
    s = wave_max(s);
    if (tid == 128) *(float*)(ws + WS_KN2 + g * 4) = s;
  }
}

constexpr int A_ONES = 65536, A_LDS = 65536 + 8192;
constexpr float QSCALE = 0.25f * 1.4426950408889634f;
constexpr float BOUND_MAX = 48.f;

__device__ __forceinline__ void pack8(const f32x16& s, int o, bf16x8& out) {
  u32x4 t = {cvtpk(s[o], s[o + 1]), cvtpk(s[o + 2], s[o + 3]), cvtpk(s[o + 4], s[o + 5]), cvtpk(s[o + 6], s[o + 7])};
  out = __builtin_bit_cast(bf16x8, t);
}

__device__ __forceinline__ void attn_role(int a_id, const u16* __restrict__ wq, char* __restrict__ ws, char* lds) {
  const int tid = threadIdx.x, lane = tid & 63, w = __builtin_amdgcn_readfirstlane(tid >> 6);
  const unsigned lb = (unsigned)(uintptr_t)lds;
  const int ip = a_id >> 3, c = a_id & 7;
  const int ct = w >> 1, tt = 2 * ip + (w & 1);
  const int hh = lane >> 5;
  {
    const unsigned voff = (lane >> 3) * 1024 + (((lane & 7) ^ (((lane >> 4) & 1) << 2)) * 16);
#pragma unroll
    for (int hf = 0; hf < 2; ++hf)
#pragma unroll
      for (int i = 0; i < 8; ++i) {
        const int p = hf * 32 + w * 8 + i;
        glds16((const char*)wq + c * 128 + p * 8192, voff, lb + p * 1024);
      }
  }
  bf16x8 xb[32];
  {
    const bf16x8* xp = (const bf16x8*)(ws + WS_XNF + (size_t)tt * 32 * 1024) + lane;
#pragma unroll
    for (int ks = 0; ks < 32; ++ks) xb[ks] = xp[ks * 64];
  }
  {
    const u32x4 one = {0x3f803f80u, 0x3f803f80u, 0x3f803f80u, 0x3f803f80u};
    *(LAS u32x4*)((LAS char*)lds + A_ONES + tid * 16) = one;
    *(LAS u32x4*)((LAS char*)lds + A_ONES + 4096 + tid * 16) = one;
  }
  float kn2;
  {
    const f32x4 k4 = *((const f32x4*)(ws + WS_KN2) + lane);
    kn2 = wave_max(fmaxf(fmaxf(k4[0], k4[1]), fmaxf(k4[2], k4[3])));
  }
  WAIT_BAR(0);
  f32x16 qacc = {};
  {
    const int gI = lane >> 4, q = (lane & 15) >> 2, p = lane & 3;
    lds_cptr ap = (lds_cptr)lds + (8 * hh + q) * 128 + ((ct ^ ((q >> 1) & 1)) * 64) + (gI & 1) * 32 + p * 8;
#pragma unroll
    for (int ks = 0; ks < 32; ++ks) qacc = MFMA32(trfrag(ap + ks * 2048, 512), xb[ks], qacc);
  }
  BAR();
  const char* kvg = ws + WS_KV;
  auto dma_chunk = [&](int ch) {
#pragma unroll
    for (int i = 0; i < 4; ++i) glds16(kvg + ch * 16384 + (w * 4 + i) * 1024, lane * 16, lb + (ch & 3) * 16384 + (w * 4 + i) * 1024);
  };
  dma_chunk(0); dma_chunk(1); dma_chunk(2);
  bf16x8 qa, qb;
  float ba, bb;
  {
#pragma unroll
    for (int r = 0; r < 16; ++r) qacc[r] *= QSCALE;
    pack8(qacc, 0, qa); pack8(qacc, 8, qb);
    const u32x4 ua = __builtin_bit_cast(u32x4, qa), ub = __builtin_bit_cast(u32x4, qb);
    float na = 0.f, nb = 0.f;
#pragma unroll
    for (int i = 0; i < 4; ++i) {
      na += bf_lo(ua[i]) * bf_lo(ua[i]) + bf_hi(ua[i]) * bf_hi(ua[i]);
      nb += bf_lo(ub[i]) * bf_lo(ub[i]) + bf_hi(ub[i]) * bf_hi(ub[i]);
    }
    na = swap32sum(na); nb = swap32sum(nb);
    ba = sqrtf(na * kn2) * 1.001f + 1e-3f; bb = sqrtf(nb * kn2) * 1.001f + 1e-3f;
  }
  const bool fast = __all(ba <= BOUND_MAX && bb <= BOUND_MAX);
  f32x16 oa = {}, ob = {};
  const unsigned koff = lane * 16;
  const int dI = lane & 31;
  const unsigned vlane = (hh * 16 + dI) * 16 + 8192;
  const unsigned vconst = A_ONES + (((lane & 31) >= 20 && (lane & 31) < 28) ? 64 : 0);
  WAIT_BAR(8);
  if (fast) {
    f32x16 ca, cb;
#pragma unroll
    for (int r = 0; r < 16; ++r) { ca[r] = -ba; cb[r] = -bb; }
    f32x16 sa0, sb0, sa1, sb1;
    { const bf16x8 k0 = ldsfrag((lds_cptr)lds + koff); sa0 = MFMA32(k0, qa, ca); sb0 = MFMA32(k0, qb, cb); }
    for (int ch = 0; ch < 8; ++ch) {
      if (ch < 6) { WAIT_BAR(4); } else { WAIT_BAR(0); }
      if (ch + 3 < 8) dma_chunk(ch + 3);
      const unsigned sl = (ch & 3) * 16384, sn = ((ch + 1) & 3) * 16384;
      lds_cptr kp = (lds_cptr)lds + sl + koff;
      lds_cptr kn = (lds_cptr)lds + sn + koff;
      lds_cptr vp = (lds_cptr)lds + (dI < 16 ? sl + vlane : vconst);
#define ATT_STEP(SA, SB, NA, NB, TAU)                                                     \
      {                                                                                   \
        const bf16x8 kx = (TAU) < 7 ? ldsfrag(kp + ((TAU) + 1) * 1024) : ldsfrag(kn);     \
        const bf16x8 v0 = ldsfrag(vp + (2 * (TAU)) * 512), v1 = ldsfrag(vp + (2 * (TAU) + 1) * 512); \
        NA = MFMA32(kx, qa, ca);                                                          \
        _Pragma("unroll") for (int r = 0; r < 16; ++r) SA[r] = __builtin_amdgcn_exp2f(SA[r]); \
        bf16x8 p0, p1;                                                                    \
        pack8(SA, 0, p0); pack8(SA, 8, p1);                                               \
        oa = MFMA32(v0, p0, oa); oa = MFMA32(v1, p1, oa);                                 \
        NB = MFMA32(kx, qb, cb);                                                          \
        _Pragma("unroll") for (int r = 0; r < 16; ++r) SB[r] = __builtin_amdgcn_exp2f(SB[r]); \
        pack8(SB, 0, p0); pack8(SB, 8, p1);                                               \
        ob = MFMA32(v0, p0, ob); ob = MFMA32(v1, p1, ob);                                 \
      }
      ATT_STEP(sa0, sb0, sa1, sb1, 0) ATT_STEP(sa1, sb1, sa0, sb0, 1)
      ATT_STEP(sa0, sb0, sa1, sb1, 2) ATT_STEP(sa1, sb1, sa0, sb0, 3)
      ATT_STEP(sa0, sb0, sa1, sb1, 4) ATT_STEP(sa1, sb1, sa0, sb0, 5)
      ATT_STEP(sa0, sb0, sa1, sb1, 6) ATT_STEP(sa1, sb1, sa0, sb0, 7)
#undef ATT_STEP
    }
  } else {
    float ma = -INFINITY, mb = -INFINITY;
    const f32x16 zero = {};
    for (int ch = 0; ch < 8; ++ch) {
      if (ch < 6) { WAIT_BAR(4); } else { WAIT_BAR(0); }
      if (ch + 3 < 8) dma_chunk(ch + 3);
      const unsigned sl = (ch & 3) * 16384;
      lds_cptr kp = (lds_cptr)lds + sl + koff;
      lds_cptr vp = (lds_cptr)lds + (dI < 16 ? sl + vlane : vconst);
      for (int tau = 0; tau < 8; ++tau) {
        const bf16x8 kx = ldsfrag(kp + tau * 1024);
        const bf16x8 v0 = ldsfrag(vp + (2 * tau) * 512), v1 = ldsfrag(vp + (2 * tau + 1) * 512);
#pragma unroll
        for (int u = 0; u < 2; ++u) {
          f32x16 s = MFMA32(kx, u ? qb : qa, zero);
          float m = s[0];
#pragma unroll
          for (int r = 1; r < 16; ++r) m = fmaxf(m, s[r]);
          auto rr = __builtin_amdgcn_permlane32_swap(__float_as_uint(m), __float_as_uint(m), false, false);
          m = fmaxf(__uint_as_float(rr[0]), __uint_as_float(rr[1]));
          const float mo = u ? mb : ma, mn = fmaxf(mo, m);
          const float al = __builtin_amdgcn_exp2f(mo - mn);
#pragma unroll
          for (int r = 0; r < 16; ++r) s[r] = __builtin_amdgcn_exp2f(s[r] - mn);
          bf16x8 p0, p1;
          pack8(s, 0, p0); pack8(s, 8, p1);
          f32x16 o = u ? ob : oa;
#pragma unroll
          for (int r = 0; r < 16; ++r) o[r] *= al;
          o = MFMA32(v0, p0, o); o = MFMA32(v1, p1, o);
          if (u) { ob = o; mb = mn; } else { oa = o; ma = mn; }
        }
      }
    }
  }
  {
    const float ra = 1.0f / oa[8], rb = 1.0f / ob[8];
#pragma unroll
    for (int r = 0; r < 8; ++r) { oa[r] *= ra; ob[r] *= rb; }
    bf16x8 pa, pb;
    pack8(oa, 0, pa); pack8(ob, 0, pb);
    bf16x8* op = (bf16x8*)(ws + WS_OF + ((size_t)tt * 32 + 4 * c + 2 * ct) * 1024) + lane;
    op[0] = pa; op[64] = pb;
  }
}

constexpr int G_UP = 7296, G_X = 14464, G_STAGE = G_X + 8192, G_LDS = 3 * G_STAGE;
__device__ __forceinline__ float gelu_mul(float g, float u) {
  const float c0 = -2.0f * 0.7978845608028654f * 1.4426950408889634f, c1 = c0 * 0.044715f;
  const float z = g * __builtin_fmaf(g * g, c1, c0);
  const float e = __builtin_amdgcn_exp2f(z);
  return g * u * __builtin_amdgcn_rcpf(1.0f + e);
}
__device__ __forceinline__ void gateup_role(int i, int j, const u16* __restrict__ wg, const u16* __restrict__ wu, char* __restrict__ ws, char* lds) {
  const int tid = threadIdx.x, lane = tid & 63, w = __builtin_amdgcn_readfirstlane(tid >> 6);
  const unsigned lb = (unsigned)(uintptr_t)lds;
  const int tt = 4 * i + w;
  unsigned voff[4];
#pragma unroll
  for (int m = 0; m < 4; ++m) {
    const int P = w + 4 * m, pp = P >= 7 ? P - 7 : P, U = pp * 64 + lane, row = U / 14, cu = U - row * 14;
    voff[m] = row * 3584 + cu * 16;
  }
  const char* xsrc = ws + WS_XNF + (size_t)tt * 32 * 1024;
  auto dma_stage = [&](int s, int slot) {
    const unsigned sb = lb + slot * G_STAGE;
#pragma unroll
    for (int m = 0; m < 4; ++m) {
      const int P = w + 4 * m;
      if (P < 14) glds16((const char*)(P >= 7 ? wu : wg) + (size_t)s * 32 * 3584 + j * 224, voff[m], sb + (P >= 7 ? G_UP + (P - 7) * 1024 : P * 1024));
    }
    glds16(xsrc + s * 2048, lane * 16, sb + G_X + w * 2048);
    glds16(xsrc + s * 2048 + 1024, lane * 16, sb + G_X + w * 2048 + 1024);
  };
  f32x16 acc[7];
#pragma unroll
  for (int t = 0; t < 7; ++t) acc[t] = (f32x16){};
  const int gI = lane >> 4, hh = gI >> 1, q = (lane & 15) >> 2, p = lane & 3;
  const unsigned aoff = (gI & 1) * G_UP + (8 * hh + q) * 224 + p * 8, boff = G_X + w * 2048 + lane * 16;
  dma_stage(0, 0); dma_stage(1, 1);
  int slot = 0;
  for (int s = 0; s < 16; ++s) {
    if (s < 15) { WAIT_BAR(5); } else { WAIT_BAR(0); }
    if (s + 2 < 16) dma_stage(s + 2, slot >= 1 ? slot - 1 : 2);
    lds_cptr ap = (lds_cptr)lds + slot * G_STAGE + aoff, bp = (lds_cptr)lds + slot * G_STAGE + boff;
#pragma unroll
    for (int kk = 0; kk < 2; ++kk) {
      const bf16x8 b = ldsfrag(bp + kk * 1024);
#pragma unroll
      for (int t = 0; t < 7; ++t) acc[t] = MFMA32(trfrag(ap + kk * 3584 + t * 32, 896), b, acc[t]);
    }
    slot = slot == 2 ? 0 : slot + 1;
  }
  u32x4* hp = (u32x4*)(ws + WS_HF + ((size_t)tt * 112 + 7 * j) * 1024) + lane;
#pragma unroll
  for (int t = 0; t < 7; ++t) {
    u32x4 o;
#pragma unroll
    for (int r = 0; r < 4; ++r) o[r] = cvtpk(gelu_mul(acc[t][2 * r], acc[t][8 + 2 * r]), gelu_mul(acc[t][2 * r + 1], acc[t][9 + 2 * r]));
    hp[t * 64] = o;
  }
}

constexpr int D_STAGE = 16384, D_LDS = 4 * D_STAGE;
__device__ __forceinline__ void down_role(int i, int cb, int kq, const u16* __restrict__ wd, char* __restrict__ ws, char* lds) {
  const int tid = threadIdx.x, lane = tid & 63, w = __builtin_amdgcn_readfirstlane(tid >> 6);
  const unsigned lb = (unsigned)(uintptr_t)lds;
  const unsigned wvoff = (lane >> 4) * 1024 + (((lane & 15) ^ ((lane >> 4) << 2)) * 16);
  const char* wsrc = (const char*)wd + (size_t)kq * 448 * 1024 + cb * 256;
  const char* hsrc = ws + WS_HF + ((size_t)(4 * i + w) * 112 + kq * 28) * 1024;
  auto dma_stage = [&](int s) {
    const unsigned sb = lb + (s & 3) * D_STAGE;
    glds16(wsrc + (size_t)(s * 32 + w * 8) * 1024, wvoff, sb + w * 2048);
    glds16(wsrc + (size_t)(s * 32 + w * 8 + 4) * 1024, wvoff, sb + w * 2048 + 1024);
    glds16(hsrc + s * 2048, lane * 16, sb + 8192 + w * 2048);
    glds16(hsrc + s * 2048 + 1024, lane * 16, sb + 8192 + w * 2048 + 1024);
  };
  const int wc = w >> 1, wt = w & 1;
  const int gI = lane >> 4, hh = gI >> 1, q = (lane & 15) >> 2, p = lane & 3;
  unsigned aoff[2];
#pragma unroll
  for (int a = 0; a < 2; ++a) aoff[a] = (4 * hh + q) * 256 + (((2 * wc + a) ^ q) * 64) + (gI & 1) * 32 + p * 8;
  const unsigned boff = 8192 + wt * 4096 + lane * 16;
  f32x16 acc[2][2];
#pragma unroll
  for (int a = 0; a < 2; ++a) { acc[a][0] = (f32x16){}; acc[a][1] = (f32x16){}; }
  dma_stage(0); dma_stage(1); dma_stage(2);
  for (int s = 0; s < 14; ++s) {
    if (s < 12) { WAIT_BAR(8); } else if (s == 12) { WAIT_BAR(4); } else { WAIT_BAR(0); }
    if (s + 3 < 14) dma_stage(s + 3);
    lds_cptr sp = (lds_cptr)lds + (s & 3) * D_STAGE;
#pragma unroll
    for (int kk = 0; kk < 2; ++kk) {
      const bf16x8 b0 = ldsfrag(sp + boff + kk * 1024), b1 = ldsfrag(sp + boff + 2048 + kk * 1024);
      const bf16x8 a0 = trfrag(sp + aoff[0] + kk * 4096, 2048), a1 = trfrag(sp + aoff[1] + kk * 4096, 2048);
      acc[0][0] = MFMA32(a0, b0, acc[0][0]); acc[0][1] = MFMA32(a0, b1, acc[0][1]);
      acc[1][0] = MFMA32(a1, b0, acc[1][0]); acc[1][1] = MFMA32(a1, b1, acc[1][1]);
    }
  }
#pragma unroll
  for (int a = 0; a < 2; ++a)
#pragma unroll
    for (int b = 0; b < 2; ++b) {
      const int tt = 4 * i + 2 * wt + b, ct = 4 * cb + 2 * wc + a;
      bf16x8* dp = (bf16x8*)(ws + WS_DP + (((size_t)kq * 64 + tt) * 16 + ct) * 2048) + lane;
      bf16x8 p0, p1;
      pack8(acc[a][b], 0, p0); pack8(acc[a][b], 8, p1);
      dp[0] = p0; dp[64] = p1;
    }
}

constexpr int K2_LDS = 65536;
__global__ __launch_bounds__(256) void k2_final(const u16* __restrict__ x, const u16* __restrict__ wo, char* __restrict__ ws, u16* __restrict__ out) {
  extern __shared__ __attribute__((aligned(16))) char lds[];
  const int tid = threadIdx.x, lane = tid & 63, w = __builtin_amdgcn_readfirstlane(tid >> 6);
  const unsigned lb = (unsigned)(uintptr_t)lds;
  const int ip = blockIdx.x >> 3, cg = blockIdx.x & 7;
  const int ctl = w >> 1, tt = 2 * ip + (w & 1), ct = 2 * cg + ctl, hh = lane >> 5;
  {
    const unsigned voff = (lane >> 3) * 1024 + (((lane & 7) ^ (((lane >> 4) & 1) << 2)) * 16);
#pragma unroll
    for (int i = 0; i < 16; ++i) { const int p = w * 16 + i; glds16((const char*)wo + cg * 128 + p * 8192, voff, lb + p * 1024); }
  }
  bf16x8 ob[32];
  {
    const bf16x8* op = (const bf16x8*)(ws + WS_OF + (size_t)tt * 32 * 1024) + lane;
#pragma unroll
    for (int ks = 0; ks < 32; ++ks) ob[ks] = op[ks * 64];
  }
  u32x4 dp[4][2];
#pragma unroll
  for (int kq = 0; kq < 4; ++kq) {
    const u32x4* p = (const u32x4*)(ws + WS_DP + (((size_t)kq * 64 + tt) * 16 + ct) * 2048) + lane;
    dp[kq][0] = p[0]; dp[kq][1] = p[64];
  }
  const int tok = tt * 32 + (lane & 31);
  u32x2 xv[4];
#pragma unroll
  for (int gq = 0; gq < 4; ++gq) xv[gq] = *(const u32x2*)(x + (size_t)tok * D_ + ct * 32 + 8 * gq + 4 * hh);
  WAIT_BAR(0);
  f32x16 acc = {};
  {
    const int gI = lane >> 4, q = (lane & 15) >> 2, p = lane & 3;
    lds_cptr ap = (lds_cptr)lds + (4 * hh + q) * 128 + ((ctl ^ ((q >> 1) & 1)) * 64) + (gI & 1) * 32 + p * 8;
#pragma unroll
    for (int ks = 0; ks < 32; ++ks) acc = MFMA32(trfrag(ap + ks * 2048, 1024), ob[ks], acc);
  }
#pragma unroll
  for (int gq = 0; gq < 4; ++gq) {
    float r[4];
#pragma unroll
    for (int e = 0; e < 4; ++e) {
      const int reg = 4 * gq + e;
      float d = 0.f;
#pragma unroll
      for (int kq = 0; kq < 4; ++kq) { const unsigned u = dp[kq][reg >> 3][(reg >> 1) & 3]; d += (reg & 1) ? bf_hi(u) : bf_lo(u); }
      const float sg = d * __builtin_amdgcn_rcpf(1.0f + __builtin_amdgcn_exp2f(-1.4426950408889634f * d));
      const unsigned xu = xv[gq][e >> 1];
      r[e] = ((e & 1) ? bf_hi(xu) : bf_lo(xu)) + acc[reg] * sg;
    }
    u32x2 o = {cvtpk(r[0], r[1]), cvtpk(r[2], r[3])};
    *(u32x2*)(out + (size_t)tok * D_ + ct * 32 + 8 * gq + 4 * hh) = o;
  }
}

__global__ __launch_bounds__(256, 2) void k1_attn(const u16* __restrict__ wq, char* __restrict__ ws) {
  extern __shared__ __attribute__((aligned(16))) char lds[];
  attn_role(blockIdx.x, wq, ws, lds);
}
__global__ __launch_bounds__(256, 2) void k1_gateup(const u16* __restrict__ wg, const u16* __restrict__ wu, char* __restrict__ ws) {
  extern __shared__ __attribute__((aligned(16))) char lds[];
  gateup_role(blockIdx.x >> 4, blockIdx.x & 15, wg, wu, ws, lds);
}
__global__ __launch_bounds__(256, 2) void k1_down(const u16* __restrict__ wd, char* __restrict__ ws) {
  extern __shared__ __attribute__((aligned(16))) char lds[];
  down_role(blockIdx.x >> 4, (blockIdx.x >> 2) & 3, blockIdx.x & 3, wd, ws, lds);
}

extern "C" void kernel_launch(void* const* d_in, const int* in_sizes, int n_in, void* d_out, int out_size, void* d_ws, size_t ws_size, hipStream_t stream) {
  const u16* x = (const u16*)d_in[0];
  const u16* wq = (const u16*)d_in[1];
  const u16* wk = (const u16*)d_in[2];
  const u16* wv = (const u16*)d_in[3];
  const u16* wo = (const u16*)d_in[4];
  const u16* wg = (const u16*)d_in[6];
  const u16* wu = (const u16*)d_in[7];
  const u16* wd = (const u16*)d_in[8];
  char* ws = (char*)d_ws;
  hipFuncSetAttribute((const void*)k1_attn, hipFuncAttributeMaxDynamicSharedMemorySize, A_LDS);
  hipFuncSetAttribute((const void*)k1_gateup, hipFuncAttributeMaxDynamicSharedMemorySize, G_LDS);
  hipFuncSetAttribute((const void*)k1_down, hipFuncAttributeMaxDynamicSharedMemorySize, D_LDS);
  hipFuncSetAttribute((const void*)k2_final, hipFuncAttributeMaxDynamicSharedMemorySize, K2_LDS);
  k0_prep<<<256, 256, 0, stream>>>(x, wk, wv, ws);
  k1_gateup<<<256, 256, G_LDS, stream>>>(wg, wu, ws);
  k1_attn<<<256, 256, A_LDS, stream>>>(wq, ws);
  k1_down<<<256, 256, D_LDS, stream>>>(wd, ws);
  k2_final<<<256, 256, K2_LDS, stream>>>(x, wo, ws, (u16*)d_out);
}
```

```cpp
#include <hip/hip_runtime.h>
#include <hip/hip_bf16.h>
#include <stdint.h>

constexpr int PRIO_FFN = 0, PRIO_ATTN = 1, PRIO_EPI = 3;

typedef unsigned short u16;
typedef __attribute__((ext_vector_type(8))) short bf16x8;
typedef __attribute__((ext_vector_type(4))) short s16x4;
typedef __attribute__((ext_vector_type(16))) float f32x16;
typedef __attribute__((ext_vector_type(4))) float f32x4;
typedef __attribute__((ext_vector_type(4))) unsigned u32x4;
typedef __attribute__((ext_vector_type(2))) unsigned u32x2;
#define LAS __attribute__((address_space(3)))
#define GAS __attribute__((address_space(1)))
typedef LAS const char* lds_cptr;
typedef GAS unsigned gu32;

constexpr int D_ = 512;
constexpr size_t WS_XNF = 0;
constexpr size_t WS_KV = 2u << 20;
constexpr size_t WS_KN2 = WS_KV + (128u << 10);
constexpr size_t WS_GEN = WS_KN2 + 4096;
constexpr size_t WS_KVF = WS_GEN + 256;
constexpr size_t WS_KVR = WS_KVF + 1024;
constexpr size_t WS_HFL = WS_KVR + 8192;
constexpr size_t WS_OFL = WS_HFL + 4096;
constexpr size_t WS_PFL = WS_OFL + 4096;
constexpr size_t WS_TMO = WS_PFL + 2048;
constexpr size_t WS_ST = WS_TMO + 1024;
constexpr size_t WS_HF = 3u << 20;
constexpr size_t WS_OF = 11u << 20;
constexpr size_t WS_DP = 13u << 20;

#define MFMA32(a, b, c) __builtin_amdgcn_mfma_f32_32x32x16_bf16(a, b, c, 0, 0, 0)

__device__ __forceinline__ unsigned cvtpk(float lo, float hi) {
  typedef __attribute__((ext_vector_type(2))) float f32x2_t;
  typedef __attribute__((ext_vector_type(2))) __bf16 bf16x2_t;
  f32x2_t v = {lo, hi};
  bf16x2_t r = __builtin_convertvector(v, bf16x2_t);
  return __builtin_bit_cast(unsigned, r);
}
__device__ __forceinline__ unsigned uni(unsigned v) { return (unsigned)__builtin_amdgcn_readfirstlane((int)v); }
__device__ __forceinline__ const char* unip(const void* p) {
  const unsigned long long v = (unsigned long long)p;
  return (const char*)(((unsigned long long)uni((unsigned)(v >> 32)) << 32) | uni((unsigned)v));
}
__device__ __forceinline__ float bf_round(float v) { return __uint_as_float(cvtpk(v, 0.f) << 16); }
__device__ __forceinline__ float bf_lo(unsigned u) { return __uint_as_float(u << 16); }
__device__ __forceinline__ float bf_hi(unsigned u) { return __uint_as_float(u & 0xffff0000u); }

__device__ __forceinline__ void glds16(const void* sbase, unsigned voff, unsigned lds_dst) {
  unsigned sv;
  asm volatile("s_mov_b32 %0, m0\n\ts_mov_b32 m0, %3\n\ts_nop 0\n\tglobal_load_lds_dwordx4 %1, %2\n\ts_mov_b32 m0, %0"
               : "=&s"(sv) : "v"(voff), "s"(sbase), "s"(lds_dst) : "memory");
}
__device__ __forceinline__ void glds16_sc1(const void* sbase, unsigned voff, unsigned lds_dst) {
  unsigned sv;
  asm volatile("s_mov_b32 %0, m0\n\ts_mov_b32 m0, %3\n\ts_nop 0\n\tglobal_load_lds_dwordx4 %1, %2 sc1\n\ts_mov_b32 m0, %0"
               : "=&s"(sv) : "v"(voff), "s"(sbase), "s"(lds_dst) : "memory");
}
__device__ __forceinline__ void wait_words(char* ws, size_t off, int n, unsigned tag, int lane) {
  gu32* p = (gu32*)(ws + off) + (lane < n ? lane : 0);
  for (unsigned spins = 0;; ++spins) {
    const unsigned v = __hip_atomic_load(p, __ATOMIC_RELAXED, __HIP_MEMORY_SCOPE_AGENT);
    if (__all(v == tag)) break;
    __builtin_amdgcn_s_sleep(2);
    if (spins > (1u << 21)) { if (lane == 0) __hip_atomic_store((gu32*)(ws + WS_TMO), tag, __ATOMIC_RELAXED, __HIP_MEMORY_SCOPE_AGENT); break; }
  }
  asm volatile("" ::: "memory");
}
__device__ __forceinline__ void wait_all256(char* ws, size_t off, unsigned tag, int lane) {
  gu32* p = (gu32*)(ws + off) + lane;
  for (unsigned spins = 0;; ++spins) {
    const unsigned v0 = __hip_atomic_load(p, __ATOMIC_RELAXED, __HIP_MEMORY_SCOPE_AGENT), v1 = __hip_atomic_load(p + 64, __ATOMIC_RELAXED, __HIP_MEMORY_SCOPE_AGENT);
    const unsigned v2 = __hip_atomic_load(p + 128, __ATOMIC_RELAXED, __HIP_MEMORY_SCOPE_AGENT), v3 = __hip_atomic_load(p + 192, __ATOMIC_RELAXED, __HIP_MEMORY_SCOPE_AGENT);
    if (__all(v0 == tag && v1 == tag && v2 == tag && v3 == tag)) break;
    __builtin_amdgcn_s_sleep(2);
    if (spins > (1u << 21)) { if (lane == 0) __hip_atomic_store((gu32*)(ws + WS_TMO), tag, __ATOMIC_RELAXED, __HIP_MEMORY_SCOPE_AGENT); break; }
  }
  asm volatile("" ::: "memory");
}
__device__ __forceinline__ void set_word(char* ws, size_t off, unsigned tag) { __hip_atomic_store((gu32*)(ws + off), tag, __ATOMIC_RELAXED, __HIP_MEMORY_SCOPE_AGENT); }
#define WAIT_BAR(n) asm volatile("s_waitcnt vmcnt(" #n ") lgkmcnt(0)\n\ts_barrier" ::: "memory")
#define BAR() asm volatile("s_waitcnt lgkmcnt(0)\n\ts_barrier" ::: "memory")

__device__ __forceinline__ s16x4 trrd(lds_cptr p) {
  typedef short v4i16_t __attribute__((ext_vector_type(4)));
  return __builtin_bit_cast(s16x4, __builtin_amdgcn_ds_read_tr16_b64_v4i16((LAS v4i16_t*)p));
}
__device__ __forceinline__ bf16x8 trfrag(lds_cptr p, int second_off) {
  s16x4 a = trrd(p), b = trrd(p + second_off);
  return (bf16x8){a[0], a[1], a[2], a[3], b[0], b[1], b[2], b[3]};
}
__device__ __forceinline__ bf16x8 ldsfrag(lds_cptr p) { return *(const LAS bf16x8*)p; }

template <int CTRL> __device__ __forceinline__ float dppf(float v) {
  return __int_as_float(__builtin_amdgcn_update_dpp(0, __float_as_int(v), CTRL, 0xf, 0xf, true));
}
__device__ __forceinline__ float swap32sum(float v) {
  auto r = __builtin_amdgcn_permlane32_swap(__float_as_uint(v), __float_as_uint(v), false, false);
  return __uint_as_float(r[0]) + __uint_as_float(r[1]);
}
__device__ __forceinline__ float wave_sum(float v) {
  v += dppf<0xB1>(v);
  v += dppf<0x4E>(v);
  v += dppf<0x141>(v);
  v += dppf<0x140>(v);
  auto r = __builtin_amdgcn_permlane16_swap(__float_as_uint(v), __float_as_uint(v), false, false);
  v = __uint_as_float(r[0]) + __uint_as_float(r[1]);
  return swap32sum(v);
}
__device__ __forceinline__ float wave_max(float v) {
  v = fmaxf(v, dppf<0xB1>(v));
  v = fmaxf(v, dppf<0x4E>(v));
  v = fmaxf(v, dppf<0x141>(v));
  v = fmaxf(v, dppf<0x140>(v));
  auto r = __builtin_amdgcn_permlane16_swap(__float_as_uint(v), __float_as_uint(v), false, false);
  v = fmaxf(__uint_as_float(r[0]), __uint_as_float(r[1]));
  auto r2 = __builtin_amdgcn_permlane32_swap(__float_as_uint(v), __float_as_uint(v), false, false);
  return fmaxf(__uint_as_float(r2[0]), __uint_as_float(r2[1]));
}
__device__ __forceinline__ void pack8(const f32x16& s, int o, bf16x8& out) {
  u32x4 t = {cvtpk(s[o], s[o + 1]), cvtpk(s[o + 2], s[o + 3]), cvtpk(s[o + 4], s[o + 5]), cvtpk(s[o + 6], s[o + 7])};
  out = __builtin_bit_cast(bf16x8, t);
}

constexpr float QSCALE = 0.25f * 1.4426950408889634f;
constexpr unsigned ONE2 = 0x3f803f80u;
constexpr int KV_XS = 0, KV_WK = 8 * 1040, KV_WV = KV_WK + 16384 + 128, KV_RED = KV_WV + 16384, KV_KVS = KV_RED + 4096, KV_XR = KV_KVS + 512, KV_LDS = KV_XR + 8192;
__device__ __forceinline__ unsigned kv_prep(int g, unsigned genv, const u16* __restrict__ x, const u16* __restrict__ wk, const u16* __restrict__ wv, char* __restrict__ ws, char* lds) {
  const int tid = threadIdx.x, lane = tid & 63, w = __builtin_amdgcn_readfirstlane(tid >> 6);
  const unsigned lb = (unsigned)(uintptr_t)lds;
#pragma unroll
  for (int r = 0; r < 2; ++r) glds16((const char*)x + (size_t)(8 * g + 2 * w + r) * 1024, lane * 16, lb + KV_XR + (2 * w + r) * 1024);
#pragma unroll
  for (int i = 0; i < 4; ++i) {
    glds16((const char*)wk + (w * 4 + i) * 1024, lane * 16, lb + KV_WK + (w * 4 + i) * 1024);
    glds16((const char*)wv + (w * 4 + i) * 1024, lane * 16, lb + KV_WV + (w * 4 + i) * 1024);
  }
  const __amdgpu_buffer_rsrc_t st_ = __builtin_amdgcn_make_buffer_rsrc(ws + WS_ST, 0, 16384, 0x00020000);
  asm volatile("s_waitcnt vmcnt(8)" ::: "memory");
  u32x4 xr[2];
#pragma unroll
  for (int r = 0; r < 2; ++r) xr[r] = *(const LAS u32x4*)((lds_cptr)lds + KV_XR + (2 * w + r) * 1024 + lane * 16);
#pragma unroll
  for (int r = 0; r < 2; ++r) {
    float v[8];
#pragma unroll
    for (int i = 0; i < 4; ++i) { v[2 * i] = bf_lo(xr[r][i]); v[2 * i + 1] = bf_hi(xr[r][i]); }
    float s = ((v[0] + v[1]) + (v[2] + v[3])) + ((v[4] + v[5]) + (v[6] + v[7]));
    const float mean32 = wave_sum(s) * (1.0f / D_);
    float q = 0.f;
#pragma unroll
    for (int i = 0; i < 8; ++i) { const float dd = v[i] - mean32; q += dd * dd; }
    const float mean = bf_round(mean32), var = bf_round(wave_sum(q) * (1.0f / D_));
    const float den = bf_round(sqrtf(bf_round(var + 1e-5f))), rs = 1.0f / den;
    u32x4 o;
#pragma unroll
    for (int i = 0; i < 4; ++i) {
      const unsigned d2 = cvtpk(v[2 * i] - mean, v[2 * i + 1] - mean);
      o[i] = cvtpk(bf_lo(d2) * rs, bf_hi(d2) * rs);
    }
    *(LAS u32x4*)((LAS char*)lds + KV_XS + (2 * w + r) * 1040 + lane * 16) = o;
    if (lane == 0) __builtin_amdgcn_raw_buffer_store_b64((u32x2){__float_as_uint(mean), __float_as_uint(rs)}, st_, (8 * g + 2 * w + r) * 8, 0, 16);
  }
  WAIT_BAR(0);
  const int tt = g >> 2, a = g & 3, t0 = a * 8;
  const __amdgpu_buffer_rsrc_t xr_ = __builtin_amdgcn_make_buffer_rsrc(ws + WS_XNF, 0, 2u << 20, 0x00020000);
  const __amdgpu_buffer_rsrc_t kr_ = __builtin_amdgcn_make_buffer_rsrc(ws + WS_KV, 0, (128u << 10) + 4096u, 0x00020000);
  {
    const int gI = lane >> 4, hh = gI >> 1, q = (lane & 15) >> 2, p = lane & 3;
    lds_cptr ap = (lds_cptr)lds + ((gI & 1) ? KV_WV : KV_WK) + (8 * hh + q) * 32 + p * 8 + w * 8 * 512;
    lds_cptr bp = (lds_cptr)lds + KV_XS + (lane & 7) * 1040 + hh * 16 + w * 8 * 32;
    f32x16 acc = {};
#pragma unroll
    for (int ks = 0; ks < 8; ++ks) acc = MFMA32(trfrag(ap + ks * 512, 128), ldsfrag(bp + ks * 32), acc);
    if ((lane & 31) < 8) {
#pragma unroll
      for (int r = 0; r < 16; ++r) *(LAS float*)((LAS char*)lds + KV_RED + (((w * 16 + r) * 2 + hh) * 8 + (lane & 7)) * 4) = acc[r];
    }
  }
#pragma unroll
  for (int i = 0; i < 2; ++i) {
    const int p = (2 * w + i) * 8 + (lane >> 3), ks = p >> 1, hh = p & 1, tok = lane & 7;
    const u32x4 v = *(const LAS u32x4*)((lds_cptr)lds + KV_XS + tok * 1040 + ks * 32 + hh * 16);
    __builtin_amdgcn_raw_buffer_store_b128(v, xr_, ((tt * 32 + ks) * 64 + hh * 32 + t0 + tok) * 16, 0, 16);
  }
  BAR();
  {
    const int n = tid >> 3, tok = tid & 7, hh = (n >> 2) & 1, r = (n & 3) + 4 * (n >> 3);
    float s = 0.f;
#pragma unroll
    for (int ww = 0; ww < 4; ++ww) s += *(const LAS float*)((lds_cptr)lds + KV_RED + (((ww * 16 + r) * 2 + hh) * 8 + tok) * 4);
    *(LAS u16*)((LAS char*)lds + KV_KVS + (tok * 32 + n) * 2) = (u16)(cvtpk(s, 0.f) & 0xffffu);
  }
  BAR();
  const LAS u16* kvs = (const LAS u16*)((lds_cptr)lds + KV_KVS);
  const unsigned kvo = (tt >> 3) * 16384;
  const int tau = tt & 7;
  if (tid < 16) {
    const int tok = tid & 7, hh = tid >> 3;
    u32x4 v;
#pragma unroll
    for (int i = 0; i < 4; ++i) {
      const int d0 = 8 * ((2 * i) >> 2) + 4 * hh + ((2 * i) & 3);
      v[i] = (unsigned)kvs[tok * 32 + d0] | ((unsigned)kvs[tok * 32 + d0 + 1] << 16);
    }
    __builtin_amdgcn_raw_buffer_store_b128(v, kr_, kvo + tau * 1024 + (hh * 32 + t0 + tok) * 16, 0, 16);
  } else if (tid >= 64 && tid < 96) {
    const int d = tid & 15, hh = (tid >> 4) & 1;
    u32x2 v;
    v[0] = (unsigned)kvs[(4 * hh + 0) * 32 + 16 + d] | ((unsigned)kvs[(4 * hh + 1) * 32 + 16 + d] << 16);
    v[1] = (unsigned)kvs[(4 * hh + 2) * 32 + 16 + d] | ((unsigned)kvs[(4 * hh + 3) * 32 + 16 + d] << 16);
    __builtin_amdgcn_raw_buffer_store_b64(v, kr_, kvo + 8192 + (tau * 2 + (a >> 1)) * 512 + (hh * 16 + d) * 16 + (a & 1) * 8, 0, 16);
  } else if (tid >= 128 && tid < 192) {
    float s = 0.f;
    const int tok = tid & 7;
#pragma unroll
    for (int d = 0; d < 16; ++d) { const float f = __uint_as_float((unsigned)kvs[tok * 32 + d] << 16); s += f * f; }
    s = wave_max(s);
    if (tid == 128) __builtin_amdgcn_raw_buffer_store_b32(__float_as_uint(s), kr_, (128u << 10) + g * 4, 0, 16);
  }
  asm volatile("s_waitcnt vmcnt(0) lgkmcnt(0)\n\ts_barrier" ::: "memory");
  const unsigned tag = uni(genv) + 1u;
  if (tid < 9) set_word(ws, (tid == 8 ? WS_KVF : WS_KVR + tid * 1024) + g * 4, tag);
  return tag;
}

constexpr int A_ONES = 65536, A_KN2 = 65536 + 8192, A_ST = A_KN2 + 1024, A_LDS = A_ST + 1024;
constexpr float BOUND_MAX = 48.f;

__device__ __forceinline__ void attn_role(int ip, int c, int dg, unsigned genv, const u16* __restrict__ x, const u16* __restrict__ wq, char* __restrict__ ws, char* lds) {
  const int tid = threadIdx.x, lane = tid & 63, w = __builtin_amdgcn_readfirstlane(tid >> 6);
  const unsigned lb = (unsigned)(uintptr_t)lds;
  const int ct = w >> 1, tt = 2 * ip + (w & 1);
  const int hh = lane >> 5;
  const char* kvg = ws + WS_KV;
  auto dma_chunk = [&](int ch) {
#pragma unroll
    for (int i = 0; i < 4; ++i) glds16_sc1(kvg + ch * 16384 + (w * 4 + i) * 1024, lane * 16, lb + (ch & 3) * 16384 + (w * 4 + i) * 1024);
  };
  bf16x8 qa, qb;
  f32x16 qacc = {}, csacc = {};
  {
    const unsigned wvoff = (lane >> 3) * 1024 + (((lane & 7) ^ (((lane >> 4) & 1) << 2)) * 16);
    const unsigned xv0 = (lane >> 3) * 1024 + (((lane & 7) ^ ((lane >> 4) & 7)) * 16), xv1 = (lane >> 3) * 1024 + (((lane & 7) ^ (((lane >> 4) + 4) & 7)) * 16);
    const char* wsrc = (const char*)wq + c * 128 + (size_t)w * 16 * 1024;
    const char* xsrc = (const char*)x + ((size_t)64 * ip + 16 * w) * 1024;
    auto dma_stage = [&](int s) {
      const unsigned sb = lb + (s & 3) * 16384;
      glds16(wsrc + (size_t)s * 65536, wvoff, sb + w * 2048);
      glds16(wsrc + (size_t)s * 65536 + 8192, wvoff, sb + w * 2048 + 1024);
      glds16(xsrc + s * 128, xv0, sb + 8192 + w * 2048);
      glds16(xsrc + s * 128 + 8192, xv1, sb + 8192 + w * 2048 + 1024);
    };
    dma_stage(0); dma_stage(1); dma_stage(2);
    {
      const u32x4 one = {ONE2, ONE2, ONE2, ONE2};
      *(LAS u32x4*)((LAS char*)lds + A_ONES + tid * 16) = one;
      *(LAS u32x4*)((LAS char*)lds + A_ONES + 4096 + tid * 16) = one;
    }
    const bf16x8 onesf = __builtin_bit_cast(bf16x8, (u32x4){ONE2, ONE2, ONE2, ONE2});
    const int gI = lane >> 4, q = (lane & 15) >> 2, p = lane & 3, xrow = 32 * (w & 1) + (lane & 31);
    const unsigned aoff = (8 * hh + q) * 128 + ((ct ^ ((q >> 1) & 1)) * 64) + (gI & 1) * 32 + p * 8;
#pragma unroll
    for (int s = 0; s < 8; ++s) {
      if (s < 6) { WAIT_BAR(8); } else if (s == 6) { WAIT_BAR(4); } else { WAIT_BAR(0); }
      if (s + 3 < 8) dma_stage(s + 3);
      lds_cptr sp = (lds_cptr)lds + (s & 3) * 16384;
      bf16x8 af[4], bf[4];
#pragma unroll
      for (int kk = 0; kk < 4; ++kk) { bf[kk] = ldsfrag(sp + 8192 + xrow * 128 + (((2 * kk + hh) ^ ((xrow >> 1) & 7)) * 16)); af[kk] = trfrag(sp + aoff + kk * 2048, 512); }
#pragma unroll
      for (int kk = 0; kk < 4; ++kk) { qacc = MFMA32(af[kk], bf[kk], qacc); csacc = MFMA32(af[kk], onesf, csacc); }
    }
  }
  const unsigned tag = uni(genv) + 1u;
  if (w == 0) wait_all256(ws, WS_KVR + (dg & 7) * 1024, tag, lane);
  BAR();
  if (w == 0) { glds16_sc1(ws + WS_KN2, lane * 16, lb + A_KN2); glds16_sc1(ws + WS_ST + ip * 512, lane * 16, lb + A_ST); }
  dma_chunk(0); dma_chunk(1); dma_chunk(2);
  WAIT_BAR(8);
  {
    const u32x2 stv = *(const LAS u32x2*)((lds_cptr)lds + A_ST + (32 * (w & 1) + (lane & 31)) * 8);
    const float mean = __uint_as_float(stv[0]), sc = __uint_as_float(stv[1]) * QSCALE;
    float o[16];
#pragma unroll
    for (int r = 0; r < 16; ++r) o[r] = (qacc[r] - mean * csacc[r]) * sc;
    qa = __builtin_bit_cast(bf16x8, (u32x4){cvtpk(o[0], o[1]), cvtpk(o[2], o[3]), cvtpk(o[4], o[5]), cvtpk(o[6], o[7])});
    qb = __builtin_bit_cast(bf16x8, (u32x4){cvtpk(o[8], o[9]), cvtpk(o[10], o[11]), cvtpk(o[12], o[13]), cvtpk(o[14], o[15])});
  }
  const u32x4 k4 = *(const LAS u32x4*)((lds_cptr)lds + A_KN2 + lane * 16);
  float ba, bb;
  {
    const float kn2 = wave_max(fmaxf(fmaxf(__uint_as_float(k4[0]), __uint_as_float(k4[1])), fmaxf(__uint_as_float(k4[2]), __uint_as_float(k4[3]))));
    const u32x4 ua = __builtin_bit_cast(u32x4, qa), ub = __builtin_bit_cast(u32x4, qb);
    float na = 0.f, nb = 0.f;
#pragma unroll
    for (int i = 0; i < 4; ++i) {
      na += bf_lo(ua[i]) * bf_lo(ua[i]) + bf_hi(ua[i]) * bf_hi(ua[i]);
      nb += bf_lo(ub[i]) * bf_lo(ub[i]) + bf_hi(ub[i]) * bf_hi(ub[i]);
    }
    na = swap32sum(na); nb = swap32sum(nb);
    ba = sqrtf(na * kn2) * 1.001f + 1e-3f; bb = sqrtf(nb * kn2) * 1.001f + 1e-3f;
  }
  const bool fast = __all(ba <= BOUND_MAX && bb <= BOUND_MAX);
  f32x16 oa = {}, ob = {};
  const unsigned koff = lane * 16;
  const int dI = lane & 31;
  const unsigned vlane = (hh * 16 + dI) * 16 + 8192;
  const unsigned vconst = A_ONES + (((lane & 31) >= 20 && (lane & 31) < 28) ? 64 : 0);
  if (fast) {
    f32x16 ca, cb;
#pragma unroll
    for (int r = 0; r < 16; ++r) { ca[r] = -ba; cb[r] = -bb; }
    f32x16 sa0, sb0;
    bf16x8 kA, vA0, vA1, vB1 = {}, p1 = {};
    unsigned knext = lb + koff, vbase = lb + (dI < 16 ? vlane : vconst), vnext = vbase, vmask = dI < 16 ? 0xffffffffu : 0u;
    {
      const bf16x8 k0 = ldsfrag((lds_cptr)lds + koff);
      kA = ldsfrag((lds_cptr)lds + koff + 1024);
      lds_cptr vp0 = (lds_cptr)lds + (dI < 16 ? vlane : vconst);
      vA0 = ldsfrag(vp0); vA1 = ldsfrag(vp0 + 512);
      sa0 = MFMA32(k0, qa, ca); sb0 = MFMA32(k0, qb, cb);
    }
    const unsigned ldsw = lb + w * 4096, kaddr = lb + koff;
    const char* kvw = kvg + w * 4096;
    asm volatile(
      "s_nop 7\n\t"
      "s_nop 7\n\t"
      "s_mov_b32 s69, m0\n\t"
      "s_mov_b32 s62, 0\n\t"
      "ATT_MAIN%=:\n\t"
      "s_waitcnt vmcnt(4) lgkmcnt(0)\n\t"
      "s_barrier\n\t"
      "s_add_u32 s66, s62, 1\n\t"
      "s_and_b32 s66, s66, 3\n\t"
      "s_lshl_b32 s66, s66, 14\n\t"
      "v_mov_b32 v170, v171\n\t"
      "v_mov_b32 v172, v173\n\t"
      "v_add_u32 v171, s66, v174\n\t"
      "v_and_b32 v177, s66, v176\n\t"
      "v_add_u32 v173, v175, v177\n\t"
      "s_add_u32 s67, s62, 3\n\t"
      "s_lshl_b32 s68, s67, 14\n\t"
      "s_add_u32 s60, s64, s68\n\t"
      "s_addc_u32 s61, s65, 0\n\t"
      "s_and_b32 s67, s67, 3\n\t"
      "s_lshl_b32 s67, s67, 14\n\t"
      "s_add_u32 s67, s67, s63\n\t"
      "s_mov_b32 m0, s67\n\t"
      "v_mfma_f32_32x32x16_bf16 v[98:113], v[138:141], v[130:133], v[34:49]\n\t"
      "ds_read_b128 v[142:145], v170 offset:2048\n\t"
      "v_exp_f32 v66, v66\n\t"
      "v_exp_f32 v67, v67\n\t"
      "v_exp_f32 v68, v68\n\t"
      "v_exp_f32 v69, v69\n\t"
      "v_mfma_f32_32x32x16_bf16 v[18:33], v[158:161], v[166:169], v[18:33]\n\t"
      "ds_read_b128 v[154:157], v172 offset:1024\n\t"
      "v_exp_f32 v70, v70\n\t"
      "v_exp_f32 v71, v71\n\t"
      "v_exp_f32 v72, v72\n\t"
      "v_exp_f32 v73, v73\n\t"
      "v_cvt_pk_bf16_f32 v162, v66, v67\n\t"
      "v_cvt_pk_bf16_f32 v163, v68, v69\n\t"
      "v_cvt_pk_bf16_f32 v164, v70, v71\n\t"
      "v_cvt_pk_bf16_f32 v165, v72, v73\n\t"
      "v_exp_f32 v74, v74\n\t"
      "v_exp_f32 v75, v75\n\t"
      "v_mfma_f32_32x32x16_bf16 v[2:17], v[146:149], v[162:165], v[2:17]\n\t"
      "ds_read_b128 v[158:161], v172 offset:1536\n\t"
      "v_exp_f32 v76, v76\n\t"
      "v_exp_f32 v77, v77\n\t"
      "v_exp_f32 v78, v78\n\t"
      "v_exp_f32 v79, v79\n\t"
      "v_exp_f32 v80, v80\n\t"
      "v_exp_f32 v81, v81\n\t"
      "v_cvt_pk_bf16_f32 v166, v74, v75\n\t"
      "v_cvt_pk_bf16_f32 v167, v76, v77\n\t"
      "v_cvt_pk_bf16_f32 v168, v78, v79\n\t"
      "v_cvt_pk_bf16_f32 v169, v80, v81\n\t"
      "v_exp_f32 v82, v82\n\t"
      "v_exp_f32 v83, v83\n\t"
      "v_mfma_f32_32x32x16_bf16 v[2:17], v[150:153], v[166:169], v[2:17]\n\t"
      "global_load_lds_dwordx4 v178, s[60:61] offset:0 sc1\n\t"
      "v_exp_f32 v84, v84\n\t"
      "v_exp_f32 v85, v85\n\t"
      "v_exp_f32 v86, v86\n\t"
      "v_exp_f32 v87, v87\n\t"
      "v_mfma_f32_32x32x16_bf16 v[114:129], v[138:141], v[134:137], v[50:65]\n\t"
      "v_exp_f32 v88, v88\n\t"
      "v_exp_f32 v89, v89\n\t"
      "v_cvt_pk_bf16_f32 v162, v82, v83\n\t"
      "v_cvt_pk_bf16_f32 v163, v84, v85\n\t"
      "v_cvt_pk_bf16_f32 v164, v86, v87\n\t"
      "v_cvt_pk_bf16_f32 v165, v88, v89\n\t"
      "v_exp_f32 v90, v90\n\t"
      "v_exp_f32 v91, v91\n\t"
      "v_mfma_f32_32x32x16_bf16 v[18:33], v[146:149], v[162:165], v[18:33]\n\t"
      "v_exp_f32 v92, v92\n\t"
      "v_exp_f32 v93, v93\n\t"
      "v_exp_f32 v94, v94\n\t"
      "v_exp_f32 v95, v95\n\t"
      "v_exp_f32 v96, v96\n\t"
      "v_exp_f32 v97, v97\n\t"
      "s_waitcnt lgkmcnt(0)\n\t"
      "v_cvt_pk_bf16_f32 v166, v90, v91\n\t"
      "v_cvt_pk_bf16_f32 v167, v92, v93\n\t"
      "v_cvt_pk_bf16_f32 v168, v94, v95\n\t"
      "v_cvt_pk_bf16_f32 v169, v96, v97\n\t"
      "v_mfma_f32_32x32x16_bf16 v[66:81], v[142:145], v[130:133], v[34:49]\n\t"
      "ds_read_b128 v[138:141], v170 offset:3072\n\t"
      "v_exp_f32 v98, v98\n\t"
      "v_exp_f32 v99, v99\n\t"
      "v_exp_f32 v100, v100\n\t"
      "v_exp_f32 v101, v101\n\t"
      "v_mfma_f32_32x32x16_bf16 v[18:33], v[150:153], v[166:169], v[18:33]\n\t"
      "ds_read_b128 v[146:149], v172 offset:2048\n\t"
      "v_exp_f32 v102, v102\n\t"
      "v_exp_f32 v103, v103\n\t"
      "v_exp_f32 v104, v104\n\t"
      "v_exp_f32 v105, v105\n\t"
      "v_cvt_pk_bf16_f32 v162, v98, v99\n\t"
      "v_cvt_pk_bf16_f32 v163, v100, v101\n\t"
      "v_cvt_pk_bf16_f32 v164, v102, v103\n\t"
      "v_cvt_pk_bf16_f32 v165, v104, v105\n\t"
      "v_exp_f32 v106, v106\n\t"
      "v_exp_f32 v107, v107\n\t"
      "v_mfma_f32_32x32x16_bf16 v[2:17], v[154:157], v[162:165], v[2:17]\n\t"
      "ds_read_b128 v[150:153], v172 offset:2560\n\t"
      "v_exp_f32 v108, v108\n\t"
      "v_exp_f32 v109, v109\n\t"
      "v_exp_f32 v110, v110\n\t"
      "v_exp_f32 v111, v111\n\t"
      "v_exp_f32 v112, v112\n\t"
      "v_exp_f32 v113, v113\n\t"
      "v_cvt_pk_bf16_f32 v166, v106, v107\n\t"
      "v_cvt_pk_bf16_f32 v167, v108, v109\n\t"
      "v_cvt_pk_bf16_f32 v168, v110, v111\n\t"
      "v_cvt_pk_bf16_f32 v169, v112, v113\n\t"
      "v_exp_f32 v114, v114\n\t"
      "v_exp_f32 v115, v115\n\t"
      "v_mfma_f32_32x32x16_bf16 v[2:17], v[158:161], v[166:169], v[2:17]\n\t"
      "global_load_lds_dwordx4 v178, s[60:61] offset:1024 sc1\n\t"
      "v_exp_f32 v116, v116\n\t"
      "v_exp_f32 v117, v117\n\t"
      "v_exp_f32 v118, v118\n\t"
      "v_exp_f32 v119, v119\n\t"
      "v_mfma_f32_32x32x16_bf16 v[82:97], v[142:145], v[134:137], v[50:65]\n\t"
      "v_exp_f32 v120, v120\n\t"
      "v_exp_f32 v121, v121\n\t"
      "v_cvt_pk_bf16_f32 v162, v114, v115\n\t"
      "v_cvt_pk_bf16_f32 v163, v116, v117\n\t"
      "v_cvt_pk_bf16_f32 v164, v118, v119\n\t"
      "v_cvt_pk_bf16_f32 v165, v120, v121\n\t"
      "v_exp_f32 v122, v122\n\t"
      "v_exp_f32 v123, v123\n\t"
      "v_mfma_f32_32x32x16_bf16 v[18:33], v[154:157], v[162:165], v[18:33]\n\t"
      "v_exp_f32 v124, v124\n\t"
      "v_exp_f32 v125, v125\n\t"
      "v_exp_f32 v126, v126\n\t"
      "v_exp_f32 v127, v127\n\t"
      "v_exp_f32 v128, v128\n\t"
      "v_exp_f32 v129, v129\n\t"
      "s_waitcnt lgkmcnt(0)\n\t"
      "v_cvt_pk_bf16_f32 v166, v122, v123\n\t"
      "v_cvt_pk_bf16_f32 v167, v124, v125\n\t"
      "v_cvt_pk_bf16_f32 v168, v126, v127\n\t"
      "v_cvt_pk_bf16_f32 v169, v128, v129\n\t"
      "v_mfma_f32_32x32x16_bf16 v[98:113], v[138:141], v[130:133], v[34:49]\n\t"
      "ds_read_b128 v[142:145], v170 offset:4096\n\t"
      "v_exp_f32 v66, v66\n\t"
      "v_exp_f32 v67, v67\n\t"
      "v_exp_f32 v68, v68\n\t"
      "v_exp_f32 v69, v69\n\t"
      "v_mfma_f32_32x32x16_bf16 v[18:33], v[158:161], v[166:169], v[18:33]\n\t"
      "ds_read_b128 v[154:157], v172 offset:3072\n\t"
      "v_exp_f32 v70, v70\n\t"
      "v_exp_f32 v71, v71\n\t"
      "v_exp_f32 v72, v72\n\t"
      "v_exp_f32 v73, v73\n\t"
      "v_cvt_pk_bf16_f32 v162, v66, v67\n\t"
      "v_cvt_pk_bf16_f32 v163, v68, v69\n\t"
      "v_cvt_pk_bf16_f32 v164, v70, v71\n\t"
      "v_cvt_pk_bf16_f32 v165, v72, v73\n\t"
      "v_exp_f32 v74, v74\n\t"
      "v_exp_f32 v75, v75\n\t"
      "v_mfma_f32_32x32x16_bf16 v[2:17], v[146:149], v[162:165], v[2:17]\n\t"
      "ds_read_b128 v[158:161], v172 offset:3584\n\t"
      "v_exp_f32 v76, v76\n\t"
      "v_exp_f32 v77, v77\n\t"
      "v_exp_f32 v78, v78\n\t"
      "v_exp_f32 v79, v79\n\t"
      "v_exp_f32 v80, v80\n\t"
      "v_exp_f32 v81, v81\n\t"
      "v_cvt_pk_bf16_f32 v166, v74, v75\n\t"
      "v_cvt_pk_bf16_f32 v167, v76, v77\n\t"
      "v_cvt_pk_bf16_f32 v168, v78, v79\n\t"
      "v_cvt_pk_bf16_f32 v169, v80, v81\n\t"
      "v_exp_f32 v82, v82\n\t"
      "v_exp_f32 v83, v83\n\t"
      "v_mfma_f32_32x32x16_bf16 v[2:17], v[150:153], v[166:169], v[2:17]\n\t"
      "global_load_lds_dwordx4 v178, s[60:61] offset:2048 sc1\n\t"
      "v_exp_f32 v84, v84\n\t"
      "v_exp_f32 v85, v85\n\t"
      "v_exp_f32 v86, v86\n\t"
      "v_exp_f32 v87, v87\n\t"
      "v_mfma_f32_32x32x16_bf16 v[114:129], v[138:141], v[134:137], v[50:65]\n\t"
      "v_exp_f32 v88, v88\n\t"
      "v_exp_f32 v89, v89\n\t"
      "v_cvt_pk_bf16_f32 v162, v82, v83\n\t"
      "v_cvt_pk_bf16_f32 v163, v84, v85\n\t"
      "v_cvt_pk_bf16_f32 v164, v86, v87\n\t"
      "v_cvt_pk_bf16_f32 v165, v88, v89\n\t"
      "v_exp_f32 v90, v90\n\t"
      "v_exp_f32 v91, v91\n\t"
      "v_mfma_f32_32x32x16_bf16 v[18:33], v[146:149], v[162:165], v[18:33]\n\t"
      "v_exp_f32 v92, v92\n\t"
      "v_exp_f32 v93, v93\n\t"
      "v_exp_f32 v94, v94\n\t"
      "v_exp_f32 v95, v95\n\t"
      "v_exp_f32 v96, v96\n\t"
      "v_exp_f32 v97, v97\n\t"
      "s_waitcnt lgkmcnt(0)\n\t"
      "v_cvt_pk_bf16_f32 v166, v90, v91\n\t"
      "v_cvt_pk_bf16_f32 v167, v92, v93\n\t"
      "v_cvt_pk_bf16_f32 v168, v94, v95\n\t"
      "v_cvt_pk_bf16_f32 v169, v96, v97\n\t"
      "v_mfma_f32_32x32x16_bf16 v[66:81], v[142:145], v[130:133], v[34:49]\n\t"
      "ds_read_b128 v[138:141], v170 offset:5120\n\t"
      "v_exp_f32 v98, v98\n\t"
      "v_exp_f32 v99, v99\n\t"
      "v_exp_f32 v100, v100\n\t"
      "v_exp_f32 v101, v101\n\t"
      "v_mfma_f32_32x32x16_bf16 v[18:33], v[150:153], v[166:169], v[18:33]\n\t"
      "ds_read_b128 v[146:149], v172 offset:4096\n\t"
      "v_exp_f32 v102, v102\n\t"
      "v_exp_f32 v103, v103\n\t"
      "v_exp_f32 v104, v104\n\t"
      "v_exp_f32 v105, v105\n\t"
      "v_cvt_pk_bf16_f32 v162, v98, v99\n\t"
      "v_cvt_pk_bf16_f32 v163, v100, v101\n\t"
      "v_cvt_pk_bf16_f32 v164, v102, v103\n\t"
      "v_cvt_pk_bf16_f32 v165, v104, v105\n\t"
      "v_exp_f32 v106, v106\n\t"
      "v_exp_f32 v107, v107\n\t"
      "v_mfma_f32_32x32x16_bf16 v[2:17], v[154:157], v[162:165], v[2:17]\n\t"
      "ds_read_b128 v[150:153], v172 offset:4608\n\t"
      "v_exp_f32 v108, v108\n\t"
      "v_exp_f32 v109, v109\n\t"
      "v_exp_f32 v110, v110\n\t"
      "v_exp_f32 v111, v111\n\t"
      "v_exp_f32 v112, v112\n\t"
      "v_exp_f32 v113, v113\n\t"
      "v_cvt_pk_bf16_f32 v166, v106, v107\n\t"
      "v_cvt_pk_bf16_f32 v167, v108, v109\n\t"
      "v_cvt_pk_bf16_f32 v168, v110, v111\n\t"
      "v_cvt_pk_bf16_f32 v169, v112, v113\n\t"
      "v_exp_f32 v114, v114\n\t"
      "v_exp_f32 v115, v115\n\t"
      "v_mfma_f32_32x32x16_bf16 v[2:17], v[158:161], v[166:169], v[2:17]\n\t"
      "global_load_lds_dwordx4 v178, s[60:61] offset:3072 sc1\n\t"
      "v_exp_f32 v116, v116\n\t"
      "v_exp_f32 v117, v117\n\t"
      "v_exp_f32 v118, v118\n\t"
      "v_exp_f32 v119, v119\n\t"
      "v_mfma_f32_32x32x16_bf16 v[82:97], v[142:145], v[134:137], v[50:65]\n\t"
      "v_exp_f32 v120, v120\n\t"
      "v_exp_f32 v121, v121\n\t"
      "v_cvt_pk_bf16_f32 v162, v114, v115\n\t"
      "v_cvt_pk_bf16_f32 v163, v116, v117\n\t"
      "v_cvt_pk_bf16_f32 v164, v118, v119\n\t"
      "v_cvt_pk_bf16_f32 v165, v120, v121\n\t"
      "v_exp_f32 v122, v122\n\t"
      "v_exp_f32 v123, v123\n\t"
      "v_mfma_f32_32x32x16_bf16 v[18:33], v[154:157], v[162:165], v[18:33]\n\t"
      "v_exp_f32 v124, v124\n\t"
      "v_exp_f32 v125, v125\n\t"
      "v_exp_f32 v126, v126\n\t"
      "v_exp_f32 v127, v127\n\t"
      "v_exp_f32 v128, v128\n\t"
      "v_exp_f32 v129, v129\n\t"
      "s_waitcnt lgkmcnt(0)\n\t"
      "v_cvt_pk_bf16_f32 v166, v122, v123\n\t"
      "v_cvt_pk_bf16_f32 v167, v124, v125\n\t"
      "v_cvt_pk_bf16_f32 v168, v126, v127\n\t"
      "v_cvt_pk_bf16_f32 v169, v128, v129\n\t"
      "v_mfma_f32_32x32x16_bf16 v[98:113], v[138:141], v[130:133], v[34:49]\n\t"
      "ds_read_b128 v[142:145], v170 offset:6144\n\t"
      "v_exp_f32 v66, v66\n\t"
      "v_exp_f32 v67, v67\n\t"
      "v_exp_f32 v68, v68\n\t"
      "v_exp_f32 v69, v69\n\t"
      "v_mfma_f32_32x32x16_bf16 v[18:33], v[158:161], v[166:169], v[18:33]\n\t"
      "ds_read_b128 v[154:157], v172 offset:5120\n\t"
      "v_exp_f32 v70, v70\n\t"
      "v_exp_f32 v71, v71\n\t"
      "v_exp_f32 v72, v72\n\t"
      "v_exp_f32 v73, v73\n\t"
      "v_cvt_pk_bf16_f32 v162, v66, v67\n\t"
      "v_cvt_pk_bf16_f32 v163, v68, v69\n\t"
      "v_cvt_pk_bf16_f32 v164, v70, v71\n\t"
      "v_cvt_pk_bf16_f32 v165, v72, v73\n\t"
      "v_exp_f32 v74, v74\n\t"
      "v_exp_f32 v75, v75\n\t"
      "v_mfma_f32_32x32x16_bf16 v[2:17], v[146:149], v[162:165], v[2:17]\n\t"
      "ds_read_b128 v[158:161], v172 offset:5632\n\t"
      "v_exp_f32 v76, v76\n\t"
      "v_exp_f32 v77, v77\n\t"
      "v_exp_f32 v78, v78\n\t"
      "v_exp_f32 v79, v79\n\t"
      "v_exp_f32 v80, v80\n\t"
      "v_exp_f32 v81, v81\n\t"
      "v_cvt_pk_bf16_f32 v166, v74, v75\n\t"
      "v_cvt_pk_bf16_f32 v167, v76, v77\n\t"
      "v_cvt_pk_bf16_f32 v168, v78, v79\n\t"
      "v_cvt_pk_bf16_f32 v169, v80, v81\n\t"
      "v_exp_f32 v82, v82\n\t"
      "v_exp_f32 v83, v83\n\t"
      "v_mfma_f32_32x32x16_bf16 v[2:17], v[150:153], v[166:169], v[2:17]\n\t"
      "v_exp_f32 v84, v84\n\t"
      "v_exp_f32 v85, v85\n\t"
      "v_exp_f32 v86, v86\n\t"
      "v_exp_f32 v87, v87\n\t"
      "v_mfma_f32_32x32x16_bf16 v[114:129], v[138:141], v[134:137], v[50:65]\n\t"
      "v_exp_f32 v88, v88\n\t"
      "v_exp_f32 v89, v89\n\t"
      "v_cvt_pk_bf16_f32 v162, v82, v83\n\t"
      "v_cvt_pk_bf16_f32 v163, v84, v85\n\t"
      "v_cvt_pk_bf16_f32 v164, v86, v87\n\t"
      "v_cvt_pk_bf16_f32 v165, v88, v89\n\t"
      "v_exp_f32 v90, v90\n\t"
      "v_exp_f32 v91, v91\n\t"
      "v_mfma_f32_32x32x16_bf16 v[18:33], v[146:149], v[162:165], v[18:33]\n\t"
      "v_exp_f32 v92, v92\n\t"
      "v_exp_f32 v93, v93\n\t"
      "v_exp_f32 v94, v94\n\t"
      "v_exp_f32 v95, v95\n\t"
      "v_exp_f32 v96, v96\n\t"
      "v_exp_f32 v97, v97\n\t"
      "s_waitcnt lgkmcnt(0)\n\t"
      "v_cvt_pk_bf16_f32 v166, v90, v91\n\t"
      "v_cvt_pk_bf16_f32 v167, v92, v93\n\t"
      "v_cvt_pk_bf16_f32 v168, v94, v95\n\t"
      "v_cvt_pk_bf16_f32 v169, v96, v97\n\t"
      "v_mfma_f32_32x32x16_bf16 v[66:81], v[142:145], v[130:133], v[34:49]\n\t"
      "ds_read_b128 v[138:141], v170 offset:7168\n\t"
      "v_exp_f32 v98, v98\n\t"
      "v_exp_f32 v99, v99\n\t"
      "v_exp_f32 v100, v100\n\t"
      "v_exp_f32 v101, v101\n\t"
      "v_mfma_f32_32x32x16_bf16 v[18:33], v[150:153], v[166:169], v[18:33]\n\t"
      "ds_read_b128 v[146:149], v172 offset:6144\n\t"
      "v_exp_f32 v102, v102\n\t"
      "v_exp_f32 v103, v103\n\t"
      "v_exp_f32 v104, v104\n\t"
      "v_exp_f32 v105, v105\n\t"
      "v_cvt_pk_bf16_f32 v162, v98, v99\n\t"
      "v_cvt_pk_bf16_f32 v163, v100, v101\n\t"
      "v_cvt_pk_bf16_f32 v164, v102, v103\n\t"
      "v_cvt_pk_bf16_f32 v165, v104, v105\n\t"
      "v_exp_f32 v106, v106\n\t"
      "v_exp_f32 v107, v107\n\t"
      "v_mfma_f32_32x32x16_bf16 v[2:17], v[154:157], v[162:165], v[2:17]\n\t"
      "ds_read_b128 v[150:153], v172 offset:6656\n\t"
      "v_exp_f32 v108, v108\n\t"
      "v_exp_f32 v109, v109\n\t"
      "v_exp_f32 v110, v110\n\t"
      "v_exp_f32 v111, v111\n\t"
      "v_exp_f32 v112, v112\n\t"
      "v_exp_f32 v113, v113\n\t"
      "v_cvt_pk_bf16_f32 v166, v106, v107\n\t"
      "v_cvt_pk_bf16_f32 v167, v108, v109\n\t"
      "v_cvt_pk_bf16_f32 v168, v110, v111\n\t"
      "v_cvt_pk_bf16_f32 v169, v112, v113\n\t"
      "v_exp_f32 v114, v114\n\t"
      "v_exp_f32 v115, v115\n\t"
      "v_mfma_f32_32x32x16_bf16 v[2:17], v[158:161], v[166:169], v[2:17]\n\t"
      "v_exp_f32 v116, v116\n\t"
      "v_exp_f32 v117, v117\n\t"
      "v_exp_f32 v118, v118\n\t"
      "v_exp_f32 v119, v119\n\t"
      "v_mfma_f32_32x32x16_bf16 v[82:97], v[142:145], v[134:137], v[50:65]\n\t"
      "v_exp_f32 v120, v120\n\t"
      "v_exp_f32 v121, v121\n\t"
      "v_cvt_pk_bf16_f32 v162, v114, v115\n\t"
      "v_cvt_pk_bf16_f32 v163, v116, v117\n\t"
      "v_cvt_pk_bf16_f32 v164, v118, v119\n\t"
      "v_cvt_pk_bf16_f32 v165, v120, v121\n\t"
      "v_exp_f32 v122, v122\n\t"
      "v_exp_f32 v123, v123\n\t"
      "v_mfma_f32_32x32x16_bf16 v[18:33], v[154:157], v[162:165], v[18:33]\n\t"
      "v_exp_f32 v124, v124\n\t"
      "v_exp_f32 v125, v125\n\t"
      "v_exp_f32 v126, v126\n\t"
      "v_exp_f32 v127, v127\n\t"
      "v_exp_f32 v128, v128\n\t"
      "v_exp_f32 v129, v129\n\t"
      "s_waitcnt lgkmcnt(0)\n\t"
      "v_cvt_pk_bf16_f32 v166, v122, v123\n\t"
      "v_cvt_pk_bf16_f32 v167, v124, v125\n\t"
      "v_cvt_pk_bf16_f32 v168, v126, v127\n\t"
      "v_cvt_pk_bf16_f32 v169, v128, v129\n\t"
      "v_mfma_f32_32x32x16_bf16 v[98:113], v[138:141], v[130:133], v[34:49]\n\t"
      "ds_read_b128 v[142:145], v171 offset:0\n\t"
      "v_exp_f32 v66, v66\n\t"
      "v_exp_f32 v67, v67\n\t"
      "v_exp_f32 v68, v68\n\t"
      "v_exp_f32 v69, v69\n\t"
      "v_mfma_f32_32x32x16_bf16 v[18:33], v[158:161], v[166:169], v[18:33]\n\t"
      "ds_read_b128 v[154:157], v172 offset:7168\n\t"
      "v_exp_f32 v70, v70\n\t"
      "v_exp_f32 v71, v71\n\t"
      "v_exp_f32 v72, v72\n\t"
      "v_exp_f32 v73, v73\n\t"
      "v_cvt_pk_bf16_f32 v162, v66, v67\n\t"
      "v_cvt_pk_bf16_f32 v163, v68, v69\n\t"
      "v_cvt_pk_bf16_f32 v164, v70, v71\n\t"
      "v_cvt_pk_bf16_f32 v165, v72, v73\n\t"
      "v_exp_f32 v74, v74\n\t"
      "v_exp_f32 v75, v75\n\t"
      "v_mfma_f32_32x32x16_bf16 v[2:17], v[146:149], v[162:165], v[2:17]\n\t"
      "ds_read_b128 v[158:161], v172 offset:7680\n\t"
      "v_exp_f32 v76, v76\n\t"
      "v_exp_f32 v77, v77\n\t"
      "v_exp_f32 v78, v78\n\t"
      "v_exp_f32 v79, v79\n\t"
      "v_exp_f32 v80, v80\n\t"
      "v_exp_f32 v81, v81\n\t"
      "v_cvt_pk_bf16_f32 v166, v74, v75\n\t"
      "v_cvt_pk_bf16_f32 v167, v76, v77\n\t"
      "v_cvt_pk_bf16_f32 v168, v78, v79\n\t"
      "v_cvt_pk_bf16_f32 v169, v80, v81\n\t"
      "v_exp_f32 v82, v82\n\t"
      "v_exp_f32 v83, v83\n\t"
      "v_mfma_f32_32x32x16_bf16 v[2:17], v[150:153], v[166:169], v[2:17]\n\t"
      "v_exp_f32 v84, v84\n\t"
      "v_exp_f32 v85, v85\n\t"
      "v_exp_f32 v86, v86\n\t"
      "v_exp_f32 v87, v87\n\t"
      "v_mfma_f32_32x32x16_bf16 v[114:129], v[138:141], v[134:137], v[50:65]\n\t"
      "v_exp_f32 v88, v88\n\t"
      "v_exp_f32 v89, v89\n\t"
      "v_cvt_pk_bf16_f32 v162, v82, v83\n\t"
      "v_cvt_pk_bf16_f32 v163, v84, v85\n\t"
      "v_cvt_pk_bf16_f32 v164, v86, v87\n\t"
      "v_cvt_pk_bf16_f32 v165, v88, v89\n\t"
      "v_exp_f32 v90, v90\n\t"
      "v_exp_f32 v91, v91\n\t"
      "v_mfma_f32_32x32x16_bf16 v[18:33], v[146:149], v[162:165], v[18:33]\n\t"
      "v_exp_f32 v92, v92\n\t"
      "v_exp_f32 v93, v93\n\t"
      "v_exp_f32 v94, v94\n\t"
      "v_exp_f32 v95, v95\n\t"
      "v_exp_f32 v96, v96\n\t"
      "v_exp_f32 v97, v97\n\t"
      "s_waitcnt lgkmcnt(0)\n\t"
      "v_cvt_pk_bf16_f32 v166, v90, v91\n\t"
      "v_cvt_pk_bf16_f32 v167, v92, v93\n\t"
      "v_cvt_pk_bf16_f32 v168, v94, v95\n\t"
      "v_cvt_pk_bf16_f32 v169, v96, v97\n\t"
      "v_mfma_f32_32x32x16_bf16 v[66:81], v[142:145], v[130:133], v[34:49]\n\t"
      "ds_read_b128 v[138:141], v171 offset:1024\n\t"
      "v_exp_f32 v98, v98\n\t"
      "v_exp_f32 v99, v99\n\t"
      "v_exp_f32 v100, v100\n\t"
      "v_exp_f32 v101, v101\n\t"
      "v_mfma_f32_32x32x16_bf16 v[18:33], v[150:153], v[166:169], v[18:33]\n\t"
      "ds_read_b128 v[146:149], v173 offset:0\n\t"
      "v_exp_f32 v102, v102\n\t"
      "v_exp_f32 v103, v103\n\t"
      "v_exp_f32 v104, v104\n\t"
      "v_exp_f32 v105, v105\n\t"
      "v_cvt_pk_bf16_f32 v162, v98, v99\n\t"
      "v_cvt_pk_bf16_f32 v163, v100, v101\n\t"
      "v_cvt_pk_bf16_f32 v164, v102, v103\n\t"
      "v_cvt_pk_bf16_f32 v165, v104, v105\n\t"
      "v_exp_f32 v106, v106\n\t"
      "v_exp_f32 v107, v107\n\t"
      "v_mfma_f32_32x32x16_bf16 v[2:17], v[154:157], v[162:165], v[2:17]\n\t"
      "ds_read_b128 v[150:153], v173 offset:512\n\t"
      "v_exp_f32 v108, v108\n\t"
      "v_exp_f32 v109, v109\n\t"
      "v_exp_f32 v110, v110\n\t"
      "v_exp_f32 v111, v111\n\t"
      "v_exp_f32 v112, v112\n\t"
      "v_exp_f32 v113, v113\n\t"
      "v_cvt_pk_bf16_f32 v166, v106, v107\n\t"
      "v_cvt_pk_bf16_f32 v167, v108, v109\n\t"
      "v_cvt_pk_bf16_f32 v168, v110, v111\n\t"
      "v_cvt_pk_bf16_f32 v169, v112, v113\n\t"
      "v_exp_f32 v114, v114\n\t"
      "v_exp_f32 v115, v115\n\t"
      "v_mfma_f32_32x32x16_bf16 v[2:17], v[158:161], v[166:169], v[2:17]\n\t"
      "v_exp_f32 v116, v116\n\t"
      "v_exp_f32 v117, v117\n\t"
      "v_exp_f32 v118, v118\n\t"
      "v_exp_f32 v119, v119\n\t"
      "v_mfma_f32_32x32x16_bf16 v[82:97], v[142:145], v[134:137], v[50:65]\n\t"
      "v_exp_f32 v120, v120\n\t"
      "v_exp_f32 v121, v121\n\t"
      "v_cvt_pk_bf16_f32 v162, v114, v115\n\t"
      "v_cvt_pk_bf16_f32 v163, v116, v117\n\t"
      "v_cvt_pk_bf16_f32 v164, v118, v119\n\t"
      "v_cvt_pk_bf16_f32 v165, v120, v121\n\t"
      "v_exp_f32 v122, v122\n\t"
      "v_exp_f32 v123, v123\n\t"
      "v_mfma_f32_32x32x16_bf16 v[18:33], v[154:157], v[162:165], v[18:33]\n\t"
      "v_exp_f32 v124, v124\n\t"
      "v_exp_f32 v125, v125\n\t"
      "v_exp_f32 v126, v126\n\t"
      "v_exp_f32 v127, v127\n\t"
      "v_exp_f32 v128, v128\n\t"
      "v_exp_f32 v129, v129\n\t"
      "s_waitcnt lgkmcnt(0)\n\t"
      "v_cvt_pk_bf16_f32 v166, v122, v123\n\t"
      "v_cvt_pk_bf16_f32 v167, v124, v125\n\t"
      "v_cvt_pk_bf16_f32 v168, v126, v127\n\t"
      "v_cvt_pk_bf16_f32 v169, v128, v129\n\t"
      "s_add_u32 s62, s62, 1\n\t"
      "s_cmp_lt_u32 s62, 5\n\t"
      "s_cbranch_scc1 ATT_MAIN%=\n\t"
      "ATT_TAIL%=:\n\t"
      "s_waitcnt vmcnt(0) lgkmcnt(0)\n\t"
      "s_barrier\n\t"
      "s_add_u32 s66, s62, 1\n\t"
      "s_and_b32 s66, s66, 3\n\t"
      "s_lshl_b32 s66, s66, 14\n\t"
      "v_mov_b32 v170, v171\n\t"
      "v_mov_b32 v172, v173\n\t"
      "v_add_u32 v171, s66, v174\n\t"
      "v_and_b32 v177, s66, v176\n\t"
      "v_add_u32 v173, v175, v177\n\t"
      "v_mfma_f32_32x32x16_bf16 v[98:113], v[138:141], v[130:133], v[34:49]\n\t"
      "ds_read_b128 v[142:145], v170 offset:2048\n\t"
      "v_exp_f32 v66, v66\n\t"
      "v_exp_f32 v67, v67\n\t"
      "v_exp_f32 v68, v68\n\t"
      "v_exp_f32 v69, v69\n\t"
      "v_mfma_f32_32x32x16_bf16 v[18:33], v[158:161], v[166:169], v[18:33]\n\t"
      "ds_read_b128 v[154:157], v172 offset:1024\n\t"
      "v_exp_f32 v70, v70\n\t"
      "v_exp_f32 v71, v71\n\t"
      "v_exp_f32 v72, v72\n\t"
      "v_exp_f32 v73, v73\n\t"
      "v_cvt_pk_bf16_f32 v162, v66, v67\n\t"
      "v_cvt_pk_bf16_f32 v163, v68, v69\n\t"
      "v_cvt_pk_bf16_f32 v164, v70, v71\n\t"
      "v_cvt_pk_bf16_f32 v165, v72, v73\n\t"
      "v_exp_f32 v74, v74\n\t"
      "v_exp_f32 v75, v75\n\t"
      "v_mfma_f32_32x32x16_bf16 v[2:17], v[146:149], v[162:165], v[2:17]\n\t"
      "ds_read_b128 v[158:161], v172 offset:1536\n\t"
      "v_exp_f32 v76, v76\n\t"
      "v_exp_f32 v77, v77\n\t"
      "v_exp_f32 v78, v78\n\t"
      "v_exp_f32 v79, v79\n\t"
      "v_exp_f32 v80, v80\n\t"
      "v_exp_f32 v81, v81\n\t"
      "v_cvt_pk_bf16_f32 v166, v74, v75\n\t"
      "v_cvt_pk_bf16_f32 v167, v76, v77\n\t"
      "v_cvt_pk_bf16_f32 v168, v78, v79\n\t"
      "v_cvt_pk_bf16_f32 v169, v80, v81\n\t"
      "v_exp_f32 v82, v82\n\t"
      "v_exp_f32 v83, v83\n\t"
      "v_mfma_f32_32x32x16_bf16 v[2:17], v[150:153], v[166:169], v[2:17]\n\t"
      "v_exp_f32 v84, v84\n\t"
      "v_exp_f32 v85, v85\n\t"
      "v_exp_f32 v86, v86\n\t"
      "v_exp_f32 v87, v87\n\t"
      "v_mfma_f32_32x32x16_bf16 v[114:129], v[138:141], v[134:137], v[50:65]\n\t"
      "v_exp_f32 v88, v88\n\t"
      "v_exp_f32 v89, v89\n\t"
      "v_cvt_pk_bf16_f32 v162, v82, v83\n\t"
      "v_cvt_pk_bf16_f32 v163, v84, v85\n\t"
      "v_cvt_pk_bf16_f32 v164, v86, v87\n\t"
      "v_cvt_pk_bf16_f32 v165, v88, v89\n\t"
      "v_exp_f32 v90, v90\n\t"
      "v_exp_f32 v91, v91\n\t"
      "v_mfma_f32_32x32x16_bf16 v[18:33], v[146:149], v[162:165], v[18:33]\n\t"
      "v_exp_f32 v92, v92\n\t"
      "v_exp_f32 v93, v93\n\t"
      "v_exp_f32 v94, v94\n\t"
      "v_exp_f32 v95, v95\n\t"
      "v_exp_f32 v96, v96\n\t"
      "v_exp_f32 v97, v97\n\t"
      "s_waitcnt lgkmcnt(0)\n\t"
      "v_cvt_pk_bf16_f32 v166, v90, v91\n\t"
      "v_cvt_pk_bf16_f32 v167, v92, v93\n\t"
      "v_cvt_pk_bf16_f32 v168, v94, v95\n\t"
      "v_cvt_pk_bf16_f32 v169, v96, v97\n\t"
      "v_mfma_f32_32x32x16_bf16 v[66:81], v[142:145], v[130:133], v[34:49]\n\t"
      "ds_read_b128 v[138:141], v170 offset:3072\n\t"
      "v_exp_f32 v98, v98\n\t"
      "v_exp_f32 v99, v99\n\t"
      "v_exp_f32 v100, v100\n\t"
      "v_exp_f32 v101, v101\n\t"
      "v_mfma_f32_32x32x16_bf16 v[18:33], v[150:153], v[166:169], v[18:33]\n\t"
      "ds_read_b128 v[146:149], v172 offset:2048\n\t"
      "v_exp_f32 v102, v102\n\t"
      "v_exp_f32 v103, v103\n\t"
      "v_exp_f32 v104, v104\n\t"
      "v_exp_f32 v105, v105\n\t"
      "v_cvt_pk_bf16_f32 v162, v98, v99\n\t"
      "v_cvt_pk_bf16_f32 v163, v100, v101\n\t"
      "v_cvt_pk_bf16_f32 v164, v102, v103\n\t"
      "v_cvt_pk_bf16_f32 v165, v104, v105\n\t"
      "v_exp_f32 v106, v106\n\t"
      "v_exp_f32 v107, v107\n\t"
      "v_mfma_f32_32x32x16_bf16 v[2:17], v[154:157], v[162:165], v[2:17]\n\t"
      "ds_read_b128 v[150:153], v172 offset:2560\n\t"
      "v_exp_f32 v108, v108\n\t"
      "v_exp_f32 v109, v109\n\t"
      "v_exp_f32 v110, v110\n\t"
      "v_exp_f32 v111, v111\n\t"
      "v_exp_f32 v112, v112\n\t"
      "v_exp_f32 v113, v113\n\t"
      "v_cvt_pk_bf16_f32 v166, v106, v107\n\t"
      "v_cvt_pk_bf16_f32 v167, v108, v109\n\t"
      "v_cvt_pk_bf16_f32 v168, v110, v111\n\t"
      "v_cvt_pk_bf16_f32 v169, v112, v113\n\t"
      "v_exp_f32 v114, v114\n\t"
      "v_exp_f32 v115, v115\n\t"
      "v_mfma_f32_32x32x16_bf16 v[2:17], v[158:161], v[166:169], v[2:17]\n\t"
      "v_exp_f32 v116, v116\n\t"
      "v_exp_f32 v117, v117\n\t"
      "v_exp_f32 v118, v118\n\t"
      "v_exp_f32 v119, v119\n\t"
      "v_mfma_f32_32x32x16_bf16 v[82:97], v[142:145], v[134:137], v[50:65]\n\t"
      "v_exp_f32 v120, v120\n\t"
      "v_exp_f32 v121, v121\n\t"
      "v_cvt_pk_bf16_f32 v162, v114, v115\n\t"
      "v_cvt_pk_bf16_f32 v163, v116, v117\n\t"
      "v_cvt_pk_bf16_f32 v164, v118, v119\n\t"
      "v_cvt_pk_bf16_f32 v165, v120, v121\n\t"
      "v_exp_f32 v122, v122\n\t"
      "v_exp_f32 v123, v123\n\t"
      "v_mfma_f32_32x32x16_bf16 v[18:33], v[154:157], v[162:165], v[18:33]\n\t"
      "v_exp_f32 v124, v124\n\t"
      "v_exp_f32 v125, v125\n\t"
      "v_exp_f32 v126, v126\n\t"
      "v_exp_f32 v127, v127\n\t"
      "v_exp_f32 v128, v128\n\t"
      "v_exp_f32 v129, v129\n\t"
      "s_waitcnt lgkmcnt(0)\n\t"
      "v_cvt_pk_bf16_f32 v166, v122, v123\n\t"
      "v_cvt_pk_bf16_f32 v167, v124, v125\n\t"
      "v_cvt_pk_bf16_f32 v168, v126, v127\n\t"
      "v_cvt_pk_bf16_f32 v169, v128, v129\n\t"
      "v_mfma_f32_32x32x16_bf16 v[98:113], v[138:141], v[130:133], v[34:49]\n\t"
      "ds_read_b128 v[142:145], v170 offset:4096\n\t"
      "v_exp_f32 v66, v66\n\t"
      "v_exp_f32 v67, v67\n\t"
      "v_exp_f32 v68, v68\n\t"
      "v_exp_f32 v69, v69\n\t"
      "v_mfma_f32_32x32x16_bf16 v[18:33], v[158:161], v[166:169], v[18:33]\n\t"
      "ds_read_b128 v[154:157], v172 offset:3072\n\t"
      "v_exp_f32 v70, v70\n\t"
      "v_exp_f32 v71, v71\n\t"
      "v_exp_f32 v72, v72\n\t"
      "v_exp_f32 v73, v73\n\t"
      "v_cvt_pk_bf16_f32 v162, v66, v67\n\t"
      "v_cvt_pk_bf16_f32 v163, v68, v69\n\t"
      "v_cvt_pk_bf16_f32 v164, v70, v71\n\t"
      "v_cvt_pk_bf16_f32 v165, v72, v73\n\t"
      "v_exp_f32 v74, v74\n\t"
      "v_exp_f32 v75, v75\n\t"
      "v_mfma_f32_32x32x16_bf16 v[2:17], v[146:149], v[162:165], v[2:17]\n\t"
      "ds_read_b128 v[158:161], v172 offset:3584\n\t"
      "v_exp_f32 v76, v76\n\t"
      "v_exp_f32 v77, v77\n\t"
      "v_exp_f32 v78, v78\n\t"
      "v_exp_f32 v79, v79\n\t"
      "v_exp_f32 v80, v80\n\t"
      "v_exp_f32 v81, v81\n\t"
      "v_cvt_pk_bf16_f32 v166, v74, v75\n\t"
      "v_cvt_pk_bf16_f32 v167, v76, v77\n\t"
      "v_cvt_pk_bf16_f32 v168, v78, v79\n\t"
      "v_cvt_pk_bf16_f32 v169, v80, v81\n\t"
      "v_exp_f32 v82, v82\n\t"
      "v_exp_f32 v83, v83\n\t"
      "v_mfma_f32_32x32x16_bf16 v[2:17], v[150:153], v[166:169], v[2:17]\n\t"
      "v_exp_f32 v84, v84\n\t"
      "v_exp_f32 v85, v85\n\t"
      "v_exp_f32 v86, v86\n\t"
      "v_exp_f32 v87, v87\n\t"
      "v_mfma_f32_32x32x16_bf16 v[114:129], v[138:141], v[134:137], v[50:65]\n\t"
      "v_exp_f32 v88, v88\n\t"
      "v_exp_f32 v89, v89\n\t"
      "v_cvt_pk_bf16_f32 v162, v82, v83\n\t"
      "v_cvt_pk_bf16_f32 v163, v84, v85\n\t"
      "v_cvt_pk_bf16_f32 v164, v86, v87\n\t"
      "v_cvt_pk_bf16_f32 v165, v88, v89\n\t"
      "v_exp_f32 v90, v90\n\t"
      "v_exp_f32 v91, v91\n\t"
      "v_mfma_f32_32x32x16_bf16 v[18:33], v[146:149], v[162:165], v[18:33]\n\t"
      "v_exp_f32 v92, v92\n\t"
      "v_exp_f32 v93, v93\n\t"
      "v_exp_f32 v94, v94\n\t"
      "v_exp_f32 v95, v95\n\t"
      "v_exp_f32 v96, v96\n\t"
      "v_exp_f32 v97, v97\n\t"
      "s_waitcnt lgkmcnt(0)\n\t"
      "v_cvt_pk_bf16_f32 v166, v90, v91\n\t"
      "v_cvt_pk_bf16_f32 v167, v92, v93\n\t"
      "v_cvt_pk_bf16_f32 v168, v94, v95\n\t"
      "v_cvt_pk_bf16_f32 v169, v96, v97\n\t"
      "v_mfma_f32_32x32x16_bf16 v[66:81], v[142:145], v[130:133], v[34:49]\n\t"
      "ds_read_b128 v[138:141], v170 offset:5120\n\t"
      "v_exp_f32 v98, v98\n\t"
      "v_exp_f32 v99, v99\n\t"
      "v_exp_f32 v100, v100\n\t"
      "v_exp_f32 v101, v101\n\t"
      "v_mfma_f32_32x32x16_bf16 v[18:33], v[150:153], v[166:169], v[18:33]\n\t"
      "ds_read_b128 v[146:149], v172 offset:4096\n\t"
      "v_exp_f32 v102, v102\n\t"
      "v_exp_f32 v103, v103\n\t"
      "v_exp_f32 v104, v104\n\t"
      "v_exp_f32 v105, v105\n\t"
      "v_cvt_pk_bf16_f32 v162, v98, v99\n\t"
      "v_cvt_pk_bf16_f32 v163, v100, v101\n\t"
      "v_cvt_pk_bf16_f32 v164, v102, v103\n\t"
      "v_cvt_pk_bf16_f32 v165, v104, v105\n\t"
      "v_exp_f32 v106, v106\n\t"
      "v_exp_f32 v107, v107\n\t"
      "v_mfma_f32_32x32x16_bf16 v[2:17], v[154:157], v[162:165], v[2:17]\n\t"
      "ds_read_b128 v[150:153], v172 offset:4608\n\t"
      "v_exp_f32 v108, v108\n\t"
      "v_exp_f32 v109, v109\n\t"
      "v_exp_f32 v110, v110\n\t"
      "v_exp_f32 v111, v111\n\t"
      "v_exp_f32 v112, v112\n\t"
      "v_exp_f32 v113, v113\n\t"
      "v_cvt_pk_bf16_f32 v166, v106, v107\n\t"
      "v_cvt_pk_bf16_f32 v167, v108, v109\n\t"
      "v_cvt_pk_bf16_f32 v168, v110, v111\n\t"
      "v_cvt_pk_bf16_f32 v169, v112, v113\n\t"
      "v_exp_f32 v114, v114\n\t"
      "v_exp_f32 v115, v115\n\t"
      "v_mfma_f32_32x32x16_bf16 v[2:17], v[158:161], v[166:169], v[2:17]\n\t"
      "v_exp_f32 v116, v116\n\t"
      "v_exp_f32 v117, v117\n\t"
      "v_exp_f32 v118, v118\n\t"
      "v_exp_f32 v119, v119\n\t"
      "v_mfma_f32_32x32x16_bf16 v[82:97], v[142:145], v[134:137], v[50:65]\n\t"
      "v_exp_f32 v120, v120\n\t"
      "v_exp_f32 v121, v121\n\t"
      "v_cvt_pk_bf16_f32 v162, v114, v115\n\t"
      "v_cvt_pk_bf16_f32 v163, v116, v117\n\t"
      "v_cvt_pk_bf16_f32 v164, v118, v119\n\t"
      "v_cvt_pk_bf16_f32 v165, v120, v121\n\t"
      "v_exp_f32 v122, v122\n\t"
      "v_exp_f32 v123, v123\n\t"
      "v_mfma_f32_32x32x16_bf16 v[18:33], v[154:157], v[162:165], v[18:33]\n\t"
      "v_exp_f32 v124, v124\n\t"
      "v_exp_f32 v125, v125\n\t"
      "v_exp_f32 v126, v126\n\t"
      "v_exp_f32 v127, v127\n\t"
      "v_exp_f32 v128, v128\n\t"
      "v_exp_f32 v129, v129\n\t"
      "s_waitcnt lgkmcnt(0)\n\t"
      "v_cvt_pk_bf16_f32 v166, v122, v123\n\t"
      "v_cvt_pk_bf16_f32 v167, v124, v125\n\t"
      "v_cvt_pk_bf16_f32 v168, v126, v127\n\t"
      "v_cvt_pk_bf16_f32 v169, v128, v129\n\t"
      "v_mfma_f32_32x32x16_bf16 v[98:113], v[138:141], v[130:133], v[34:49]\n\t"
      "ds_read_b128 v[142:145], v170 offset:6144\n\t"
      "v_exp_f32 v66, v66\n\t"
      "v_exp_f32 v67, v67\n\t"
      "v_exp_f32 v68, v68\n\t"
      "v_exp_f32 v69, v69\n\t"
      "v_mfma_f32_32x32x16_bf16 v[18:33], v[158:161], v[166:169], v[18:33]\n\t"
      "ds_read_b128 v[154:157], v172 offset:5120\n\t"
      "v_exp_f32 v70, v70\n\t"
      "v_exp_f32 v71, v71\n\t"
      "v_exp_f32 v72, v72\n\t"
      "v_exp_f32 v73, v73\n\t"
      "v_cvt_pk_bf16_f32 v162, v66, v67\n\t"
      "v_cvt_pk_bf16_f32 v163, v68, v69\n\t"
      "v_cvt_pk_bf16_f32 v164, v70, v71\n\t"
      "v_cvt_pk_bf16_f32 v165, v72, v73\n\t"
      "v_exp_f32 v74, v74\n\t"
      "v_exp_f32 v75, v75\n\t"
      "v_mfma_f32_32x32x16_bf16 v[2:17], v[146:149], v[162:165], v[2:17]\n\t"
      "ds_read_b128 v[158:161], v172 offset:5632\n\t"
      "v_exp_f32 v76, v76\n\t"
      "v_exp_f32 v77, v77\n\t"
      "v_exp_f32 v78, v78\n\t"
      "v_exp_f32 v79, v79\n\t"
      "v_exp_f32 v80, v80\n\t"
      "v_exp_f32 v81, v81\n\t"
      "v_cvt_pk_bf16_f32 v166, v74, v75\n\t"
      "v_cvt_pk_bf16_f32 v167, v76, v77\n\t"
      "v_cvt_pk_bf16_f32 v168, v78, v79\n\t"
      "v_cvt_pk_bf16_f32 v169, v80, v81\n\t"
      "v_exp_f32 v82, v82\n\t"
      "v_exp_f32 v83, v83\n\t"
      "v_mfma_f32_32x32x16_bf16 v[2:17], v[150:153], v[166:169], v[2:17]\n\t"
      "v_exp_f32 v84, v84\n\t"
      "v_exp_f32 v85, v85\n\t"
      "v_exp_f32 v86, v86\n\t"
      "v_exp_f32 v87, v87\n\t"
      "v_mfma_f32_32x32x16_bf16 v[114:129], v[138:141], v[134:137], v[50:65]\n\t"
      "v_exp_f32 v88, v88\n\t"
      "v_exp_f32 v89, v89\n\t"
      "v_cvt_pk_bf16_f32 v162, v82, v83\n\t"
      "v_cvt_pk_bf16_f32 v163, v84, v85\n\t"
      "v_cvt_pk_bf16_f32 v164, v86, v87\n\t"
      "v_cvt_pk_bf16_f32 v165, v88, v89\n\t"
      "v_exp_f32 v90, v90\n\t"
      "v_exp_f32 v91, v91\n\t"
      "v_mfma_f32_32x32x16_bf16 v[18:33], v[146:149], v[162:165], v[18:33]\n\t"
      "v_exp_f32 v92, v92\n\t"
      "v_exp_f32 v93, v93\n\t"
      "v_exp_f32 v94, v94\n\t"
      "v_exp_f32 v95, v95\n\t"
      "v_exp_f32 v96, v96\n\t"
      "v_exp_f32 v97, v97\n\t"
      "s_waitcnt lgkmcnt(0)\n\t"
      "v_cvt_pk_bf16_f32 v166, v90, v91\n\t"
      "v_cvt_pk_bf16_f32 v167, v92, v93\n\t"
      "v_cvt_pk_bf16_f32 v168, v94, v95\n\t"
      "v_cvt_pk_bf16_f32 v169, v96, v97\n\t"
      "v_mfma_f32_32x32x16_bf16 v[66:81], v[142:145], v[130:133], v[34:49]\n\t"
      "ds_read_b128 v[138:141], v170 offset:7168\n\t"
      "v_exp_f32 v98, v98\n\t"
      "v_exp_f32 v99, v99\n\t"
      "v_exp_f32 v100, v100\n\t"
      "v_exp_f32 v101, v101\n\t"
      "v_mfma_f32_32x32x16_bf16 v[18:33], v[150:153], v[166:169], v[18:33]\n\t"
      "ds_read_b128 v[146:149], v172 offset:6144\n\t"
      "v_exp_f32 v102, v102\n\t"
      "v_exp_f32 v103, v103\n\t"
      "v_exp_f32 v104, v104\n\t"
      "v_exp_f32 v105, v105\n\t"
      "v_cvt_pk_bf16_f32 v162, v98, v99\n\t"
      "v_cvt_pk_bf16_f32 v163, v100, v101\n\t"
      "v_cvt_pk_bf16_f32 v164, v102, v103\n\t"
      "v_cvt_pk_bf16_f32 v165, v104, v105\n\t"
      "v_exp_f32 v106, v106\n\t"
      "v_exp_f32 v107, v107\n\t"
      "v_mfma_f32_32x32x16_bf16 v[2:17], v[154:157], v[162:165], v[2:17]\n\t"
      "ds_read_b128 v[150:153], v172 offset:6656\n\t"
      "v_exp_f32 v108, v108\n\t"
      "v_exp_f32 v109, v109\n\t"
      "v_exp_f32 v110, v110\n\t"
      "v_exp_f32 v111, v111\n\t"
      "v_exp_f32 v112, v112\n\t"
      "v_exp_f32 v113, v113\n\t"
      "v_cvt_pk_bf16_f32 v166, v106, v107\n\t"
      "v_cvt_pk_bf16_f32 v167, v108, v109\n\t"
      "v_cvt_pk_bf16_f32 v168, v110, v111\n\t"
      "v_cvt_pk_bf16_f32 v169, v112, v113\n\t"
      "v_exp_f32 v114, v114\n\t"
      "v_exp_f32 v115, v115\n\t"
      "v_mfma_f32_32x32x16_bf16 v[2:17], v[158:161], v[166:169], v[2:17]\n\t"
      "v_exp_f32 v116, v116\n\t"
      "v_exp_f32 v117, v117\n\t"
      "v_exp_f32 v118, v118\n\t"
      "v_exp_f32 v119, v119\n\t"
      "v_mfma_f32_32x32x16_bf16 v[82:97], v[142:145], v[134:137], v[50:65]\n\t"
      "v_exp_f32 v120, v120\n\t"
      "v_exp_f32 v121, v121\n\t"
      "v_cvt_pk_bf16_f32 v162, v114, v115\n\t"
      "v_cvt_pk_bf16_f32 v163, v116, v117\n\t"
      "v_cvt_pk_bf16_f32 v164, v118, v119\n\t"
      "v_cvt_pk_bf16_f32 v165, v120, v121\n\t"
      "v_exp_f32 v122, v122\n\t"
      "v_exp_f32 v123, v123\n\t"
      "v_mfma_f32_32x32x16_bf16 v[18:33], v[154:157], v[162:165], v[18:33]\n\t"
      "v_exp_f32 v124, v124\n\t"
      "v_exp_f32 v125, v125\n\t"
      "v_exp_f32 v126, v126\n\t"
      "v_exp_f32 v127, v127\n\t"
      "v_exp_f32 v128, v128\n\t"
      "v_exp_f32 v129, v129\n\t"
      "s_waitcnt lgkmcnt(0)\n\t"
      "v_cvt_pk_bf16_f32 v166, v122, v123\n\t"
      "v_cvt_pk_bf16_f32 v167, v124, v125\n\t"
      "v_cvt_pk_bf16_f32 v168, v126, v127\n\t"
      "v_cvt_pk_bf16_f32 v169, v128, v129\n\t"
      "v_mfma_f32_32x32x16_bf16 v[98:113], v[138:141], v[130:133], v[34:49]\n\t"
      "ds_read_b128 v[142:145], v171 offset:0\n\t"
      "v_exp_f32 v66, v66\n\t"
      "v_exp_f32 v67, v67\n\t"
      "v_exp_f32 v68, v68\n\t"
      "v_exp_f32 v69, v69\n\t"
      "v_mfma_f32_32x32x16_bf16 v[18:33], v[158:161], v[166:169], v[18:33]\n\t"
      "ds_read_b128 v[154:157], v172 offset:7168\n\t"
      "v_exp_f32 v70, v70\n\t"
      "v_exp_f32 v71, v71\n\t"
      "v_exp_f32 v72, v72\n\t"
      "v_exp_f32 v73, v73\n\t"
      "v_cvt_pk_bf16_f32 v162, v66, v67\n\t"
      "v_cvt_pk_bf16_f32 v163, v68, v69\n\t"
      "v_cvt_pk_bf16_f32 v164, v70, v71\n\t"
      "v_cvt_pk_bf16_f32 v165, v72, v73\n\t"
      "v_exp_f32 v74, v74\n\t"
      "v_exp_f32 v75, v75\n\t"
      "v_mfma_f32_32x32x16_bf16 v[2:17], v[146:149], v[162:165], v[2:17]\n\t"
      "ds_read_b128 v[158:161], v172 offset:7680\n\t"
      "v_exp_f32 v76, v76\n\t"
      "v_exp_f32 v77, v77\n\t"
      "v_exp_f32 v78, v78\n\t"
      "v_exp_f32 v79, v79\n\t"
      "v_exp_f32 v80, v80\n\t"
      "v_exp_f32 v81, v81\n\t"
      "v_cvt_pk_bf16_f32 v166, v74, v75\n\t"
      "v_cvt_pk_bf16_f32 v167, v76, v77\n\t"
      "v_cvt_pk_bf16_f32 v168, v78, v79\n\t"
      "v_cvt_pk_bf16_f32 v169, v80, v81\n\t"
      "v_exp_f32 v82, v82\n\t"
      "v_exp_f32 v83, v83\n\t"
      "v_mfma_f32_32x32x16_bf16 v[2:17], v[150:153], v[166:169], v[2:17]\n\t"
      "v_exp_f32 v84, v84\n\t"
      "v_exp_f32 v85, v85\n\t"
      "v_exp_f32 v86, v86\n\t"
      "v_exp_f32 v87, v87\n\t"
      "v_mfma_f32_32x32x16_bf16 v[114:129], v[138:141], v[134:137], v[50:65]\n\t"
      "v_exp_f32 v88, v88\n\t"
      "v_exp_f32 v89, v89\n\t"
      "v_cvt_pk_bf16_f32 v162, v82, v83\n\t"
      "v_cvt_pk_bf16_f32 v163, v84, v85\n\t"
      "v_cvt_pk_bf16_f32 v164, v86, v87\n\t"
      "v_cvt_pk_bf16_f32 v165, v88, v89\n\t"
      "v_exp_f32 v90, v90\n\t"
      "v_exp_f32 v91, v91\n\t"
      "v_mfma_f32_32x32x16_bf16 v[18:33], v[146:149], v[162:165], v[18:33]\n\t"
      "v_exp_f32 v92, v92\n\t"
      "v_exp_f32 v93, v93\n\t"
      "v_exp_f32 v94, v94\n\t"
      "v_exp_f32 v95, v95\n\t"
      "v_exp_f32 v96, v96\n\t"
      "v_exp_f32 v97, v97\n\t"
      "s_waitcnt lgkmcnt(0)\n\t"
      "v_cvt_pk_bf16_f32 v166, v90, v91\n\t"
      "v_cvt_pk_bf16_f32 v167, v92, v93\n\t"
      "v_cvt_pk_bf16_f32 v168, v94, v95\n\t"
      "v_cvt_pk_bf16_f32 v169, v96, v97\n\t"
      "v_mfma_f32_32x32x16_bf16 v[66:81], v[142:145], v[130:133], v[34:49]\n\t"
      "ds_read_b128 v[138:141], v171 offset:1024\n\t"
      "v_exp_f32 v98, v98\n\t"
      "v_exp_f32 v99, v99\n\t"
      "v_exp_f32 v100, v100\n\t"
      "v_exp_f32 v101, v101\n\t"
      "v_mfma_f32_32x32x16_bf16 v[18:33], v[150:153], v[166:169], v[18:33]\n\t"
      "ds_read_b128 v[146:149], v173 offset:0\n\t"
      "v_exp_f32 v102, v102\n\t"
      "v_exp_f32 v103, v103\n\t"
      "v_exp_f32 v104, v104\n\t"
      "v_exp_f32 v105, v105\n\t"
      "v_cvt_pk_bf16_f32 v162, v98, v99\n\t"
      "v_cvt_pk_bf16_f32 v163, v100, v101\n\t"
      "v_cvt_pk_bf16_f32 v164, v102, v103\n\t"
      "v_cvt_pk_bf16_f32 v165, v104, v105\n\t"
      "v_exp_f32 v106, v106\n\t"
      "v_exp_f32 v107, v107\n\t"
      "v_mfma_f32_32x32x16_bf16 v[2:17], v[154:157], v[162:165], v[2:17]\n\t"
      "ds_read_b128 v[150:153], v173 offset:512\n\t"
      "v_exp_f32 v108, v108\n\t"
      "v_exp_f32 v109, v109\n\t"
      "v_exp_f32 v110, v110\n\t"
      "v_exp_f32 v111, v111\n\t"
      "v_exp_f32 v112, v112\n\t"
      "v_exp_f32 v113, v113\n\t"
      "v_cvt_pk_bf16_f32 v166, v106, v107\n\t"
      "v_cvt_pk_bf16_f32 v167, v108, v109\n\t"
      "v_cvt_pk_bf16_f32 v168, v110, v111\n\t"
      "v_cvt_pk_bf16_f32 v169, v112, v113\n\t"
      "v_exp_f32 v114, v114\n\t"
      "v_exp_f32 v115, v115\n\t"
      "v_mfma_f32_32x32x16_bf16 v[2:17], v[158:161], v[166:169], v[2:17]\n\t"
      "v_exp_f32 v116, v116\n\t"
      "v_exp_f32 v117, v117\n\t"
      "v_exp_f32 v118, v118\n\t"
      "v_exp_f32 v119, v119\n\t"
      "v_mfma_f32_32x32x16_bf16 v[82:97], v[142:145], v[134:137], v[50:65]\n\t"
      "v_exp_f32 v120, v120\n\t"
      "v_exp_f32 v121, v121\n\t"
      "v_cvt_pk_bf16_f32 v162, v114, v115\n\t"
      "v_cvt_pk_bf16_f32 v163, v116, v117\n\t"
      "v_cvt_pk_bf16_f32 v164, v118, v119\n\t"
      "v_cvt_pk_bf16_f32 v165, v120, v121\n\t"
      "v_exp_f32 v122, v122\n\t"
      "v_exp_f32 v123, v123\n\t"
      "v_mfma_f32_32x32x16_bf16 v[18:33], v[154:157], v[162:165], v[18:33]\n\t"
      "v_exp_f32 v124, v124\n\t"
      "v_exp_f32 v125, v125\n\t"
      "v_exp_f32 v126, v126\n\t"
      "v_exp_f32 v127, v127\n\t"
      "v_exp_f32 v128, v128\n\t"
      "v_exp_f32 v129, v129\n\t"
      "s_waitcnt lgkmcnt(0)\n\t"
      "v_cvt_pk_bf16_f32 v166, v122, v123\n\t"
      "v_cvt_pk_bf16_f32 v167, v124, v125\n\t"
      "v_cvt_pk_bf16_f32 v168, v126, v127\n\t"
      "v_cvt_pk_bf16_f32 v169, v128, v129\n\t"
      "s_add_u32 s62, s62, 1\n\t"
      "s_cmp_lt_u32 s62, 8\n\t"
      "s_cbranch_scc1 ATT_TAIL%=\n\t"
      "s_nop 1\n\t"
      "v_mfma_f32_32x32x16_bf16 v[18:33], v[158:161], v[166:169], v[18:33]\n\t"
      "s_mov_b32 m0, s69\n\t"
      "s_nop 7\n\t"
      "s_nop 7\n\t"
      : "+{v[2:17]}"(oa), "+{v[18:33]}"(ob), "+{v[66:81]}"(sa0), "+{v[82:97]}"(sb0), "+{v[138:141]}"(kA), "+{v[146:149]}"(vA0), "+{v[150:153]}"(vA1),
        "+{v[158:161]}"(vB1), "+{v[166:169]}"(p1), "+{v171}"(knext), "+{v173}"(vnext)
      : "{v[34:49]}"(ca), "{v[50:65]}"(cb), "{v[130:133]}"(qa), "{v[134:137]}"(qb), "{v174}"(kaddr), "{v175}"(vbase), "{v176}"(vmask), "{v178}"(koff),
        "{s63}"(ldsw), "{s[64:65]}"(kvw)
      : "v98", "v99", "v100", "v101", "v102", "v103", "v104", "v105", "v106", "v107", "v108", "v109", "v110", "v111", "v112", "v113", "v114", "v115", "v116", "v117", "v118", "v119", "v120", "v121", "v122", "v123", "v124", "v125", "v126", "v127", "v128", "v129", "v142", "v143", "v144", "v145", "v154", "v155", "v156", "v157", "v162", "v163", "v164", "v165", "v170", "v172", "v177", "s60", "s61", "s62", "s66", "s67", "s68", "s69", "scc", "memory");
  } else {
    float ma = -INFINITY, mb = -INFINITY;
    const f32x16 zero = {};
    for (int ch = 0; ch < 8; ++ch) {
      if (ch < 5) { WAIT_BAR(4); } else { WAIT_BAR(0); }
      if (ch + 3 < 8) dma_chunk(ch + 3);
      const unsigned sl = (ch & 3) * 16384;
      lds_cptr kp = (lds_cptr)lds + sl + koff;
      lds_cptr vp = (lds_cptr)lds + (dI < 16 ? sl + vlane : vconst);
      for (int tau = 0; tau < 8; ++tau) {
        const bf16x8 kx = ldsfrag(kp + tau * 1024);
        const bf16x8 v0 = ldsfrag(vp + (2 * tau) * 512), v1 = ldsfrag(vp + (2 * tau + 1) * 512);
#pragma unroll
        for (int u = 0; u < 2; ++u) {
          f32x16 s = MFMA32(kx, u ? qb : qa, zero);
          float m = s[0];
#pragma unroll
          for (int r = 1; r < 16; ++r) m = fmaxf(m, s[r]);
          auto rr = __builtin_amdgcn_permlane32_swap(__float_as_uint(m), __float_as_uint(m), false, false);
          m = fmaxf(__uint_as_float(rr[0]), __uint_as_float(rr[1]));
          const float mo = u ? mb : ma, mn = fmaxf(mo, m);
          const float al = __builtin_amdgcn_exp2f(mo - mn);
#pragma unroll
          for (int r = 0; r < 16; ++r) s[r] = __builtin_amdgcn_exp2f(s[r] - mn);
          bf16x8 p0, p1;
          pack8(s, 0, p0); pack8(s, 8, p1);
          f32x16 o = u ? ob : oa;
#pragma unroll
          for (int r = 0; r < 16; ++r) o[r] *= al;
          o = MFMA32(v0, p0, o); o = MFMA32(v1, p1, o);
          if (u) { ob = o; mb = mn; } else { oa = o; ma = mn; }
        }
      }
    }
  }
  {
    const float ra = __builtin_amdgcn_rcpf(oa[8]), rb = __builtin_amdgcn_rcpf(ob[8]);
#pragma unroll
    for (int r = 0; r < 8; ++r) { oa[r] *= ra; ob[r] *= rb; }
    bf16x8 pa, pb;
    pack8(oa, 0, pa); pack8(ob, 0, pb);
    const __amdgpu_buffer_rsrc_t orr = __builtin_amdgcn_make_buffer_rsrc(ws + WS_OF, 0, 64 * 32 * 1024, 0x00020000);
    const unsigned ooff = (tt * 32 + 4 * c + 2 * ct) * 1024 + lane * 16;
    __builtin_amdgcn_raw_buffer_store_b128(__builtin_bit_cast(u32x4, pa), orr, ooff, 0, 16);
    __builtin_amdgcn_raw_buffer_store_b128(__builtin_bit_cast(u32x4, pb), orr, ooff + 1024, 0, 16);
    asm volatile("s_waitcnt vmcnt(0)" ::: "memory");
    if (lane == 0) set_word(ws, WS_OFL + (tt * 16 + 2 * c + ct) * 4, tag);
  }
}

constexpr int G_UP = 7296, G_SZ = 14464, F_LDS = 5 * G_SZ;
typedef __attribute__((ext_vector_type(2))) float f32x2;
__device__ __forceinline__ unsigned gelu_mul2(f32x2 g, f32x2 u) {
  const float c0 = -2.0f * 0.7978845608028654f * 1.4426950408889634f, c1 = c0 * 0.044715f;
  const f32x2 p = __builtin_elementwise_fma(g * g, (f32x2){c1, c1}, (f32x2){c0, c0});
  const f32x2 z = g * p;
  const f32x2 d = (f32x2){__builtin_amdgcn_exp2f(z[0]), __builtin_amdgcn_exp2f(z[1])} + (f32x2){1.0f, 1.0f};
  const f32x2 h = (g * u) * (f32x2){__builtin_amdgcn_rcpf(d[0]), __builtin_amdgcn_rcpf(d[1])};
  return cvtpk(h[0], h[1]);
}
__device__ __forceinline__ void gateup_role(int tb, int j, size_t flag_off, unsigned tag, const u16* __restrict__ wg, const u16* __restrict__ wu, char* __restrict__ ws, char* lds) {
  const int tid = threadIdx.x, lane = tid & 63, w = __builtin_amdgcn_readfirstlane(tid >> 6);
  const unsigned lb = (unsigned)(uintptr_t)lds;
  const int tt = 4 * tb + w;
  unsigned voff[4], lpo[4];
  const char* wb[4];
#pragma unroll
  for (int m = 0; m < 4; ++m) {
    const int P = min(w + 4 * m, 13), pp = P >= 7 ? P - 7 : P, U = pp * 64 + lane, row = U / 14, cu = U - row * 14;
    voff[m] = row * 3584 + cu * 16;
    lpo[m] = uni(lb + (P >= 7 ? G_UP + (P - 7) * 1024 : P * 1024));
    wb[m] = unip((const char*)(P >= 7 ? wu : wg) + j * 224);
  }
  const char* xsrc = unip(ws + WS_XNF + (size_t)tt * 32 * 1024);
  const int gI = lane >> 4, hh = gI >> 1, q = (lane & 15) >> 2, p = lane & 3;
  const unsigned alane = lb + (gI & 1) * G_UP + (8 * hh + q) * 224 + p * 8, vb = lane * 16;
  const unsigned has4 = uni(w < 2 ? 1u : 0u);
  unsigned s54c;
  asm volatile(
      "s_nop 4\n\t"
      "s_mov_b32 s59, m0\n\t"
      "s_mov_b32 s54, 0\n\t"
      "s_add_u32 m0, s50, s54\n\t"
      "s_nop 0\n\t"
      "global_load_lds_dwordx4 v163, s[40:41]\n\t"
      "s_add_u32 s40, s40, 0x1c000\n\t"
      "s_addc_u32 s41, s41, 0\n\t"
      "s_add_u32 m0, s51, s54\n\t"
      "s_nop 0\n\t"
      "global_load_lds_dwordx4 v164, s[42:43]\n\t"
      "s_add_u32 s42, s42, 0x1c000\n\t"
      "s_addc_u32 s43, s43, 0\n\t"
      "s_add_u32 m0, s52, s54\n\t"
      "s_nop 0\n\t"
      "global_load_lds_dwordx4 v165, s[44:45]\n\t"
      "s_add_u32 s44, s44, 0x1c000\n\t"
      "s_addc_u32 s45, s45, 0\n\t"
      "s_cmp_eq_u32 s57, 0\n\t"
      "s_cbranch_scc1 GSK1_%=\n\t"
      "s_add_u32 m0, s53, s54\n\t"
      "s_nop 0\n\t"
      "global_load_lds_dwordx4 v166, s[46:47]\n\t"
      "s_add_u32 s46, s46, 0x1c000\n\t"
      "s_addc_u32 s47, s47, 0\n\t"
      "GSK1_%=:\n\t"
      "s_add_u32 s54, s54, 14464\n\t"
      "s_cmp_eq_u32 s54, 72320\n\t"
      "s_cselect_b32 s54, 0, s54\n\t"
      "s_add_u32 m0, s50, s54\n\t"
      "s_nop 0\n\t"
      "global_load_lds_dwordx4 v163, s[40:41]\n\t"
      "s_add_u32 s40, s40, 0x1c000\n\t"
      "s_addc_u32 s41, s41, 0\n\t"
      "s_add_u32 m0, s51, s54\n\t"
      "s_nop 0\n\t"
      "global_load_lds_dwordx4 v164, s[42:43]\n\t"
      "s_add_u32 s42, s42, 0x1c000\n\t"
      "s_addc_u32 s43, s43, 0\n\t"
      "s_add_u32 m0, s52, s54\n\t"
      "s_nop 0\n\t"
      "global_load_lds_dwordx4 v165, s[44:45]\n\t"
      "s_add_u32 s44, s44, 0x1c000\n\t"
      "s_addc_u32 s45, s45, 0\n\t"
      "s_cmp_eq_u32 s57, 0\n\t"
      "s_cbranch_scc1 GSK2_%=\n\t"
      "s_add_u32 m0, s53, s54\n\t"
      "s_nop 0\n\t"
      "global_load_lds_dwordx4 v166, s[46:47]\n\t"
      "s_add_u32 s46, s46, 0x1c000\n\t"
      "s_addc_u32 s47, s47, 0\n\t"
      "GSK2_%=:\n\t"
      "s_add_u32 s54, s54, 14464\n\t"
      "s_cmp_eq_u32 s54, 72320\n\t"
      "s_cselect_b32 s54, 0, s54\n\t"
      "s_add_u32 m0, s50, s54\n\t"
      "s_nop 0\n\t"
      "global_load_lds_dwordx4 v163, s[40:41]\n\t"
      "s_add_u32 s40, s40, 0x1c000\n\t"
      "s_addc_u32 s41, s41, 0\n\t"
      "s_add_u32 m0, s51, s54\n\t"
      "s_nop 0\n\t"
      "global_load_lds_dwordx4 v164, s[42:43]\n\t"
      "s_add_u32 s42, s42, 0x1c000\n\t"
      "s_addc_u32 s43, s43, 0\n\t"
      "s_add_u32 m0, s52, s54\n\t"
      "s_nop 0\n\t"
      "global_load_lds_dwordx4 v165, s[44:45]\n\t"
      "s_add_u32 s44, s44, 0x1c000\n\t"
      "s_addc_u32 s45, s45, 0\n\t"
      "s_cmp_eq_u32 s57, 0\n\t"
      "s_cbranch_scc1 GSK3_%=\n\t"
      "s_add_u32 m0, s53, s54\n\t"
      "s_nop 0\n\t"
      "global_load_lds_dwordx4 v166, s[46:47]\n\t"
      "s_add_u32 s46, s46, 0x1c000\n\t"
      "s_addc_u32 s47, s47, 0\n\t"
      "GSK3_%=:\n\t"
      "s_add_u32 s54, s54, 14464\n\t"
      "s_cmp_eq_u32 s54, 72320\n\t"
      "s_cselect_b32 s54, 0, s54\n\t"
      "s_add_u32 m0, s50, s54\n\t"
      "s_nop 0\n\t"
      "global_load_lds_dwordx4 v163, s[40:41]\n\t"
      "s_add_u32 s40, s40, 0x1c000\n\t"
      "s_addc_u32 s41, s41, 0\n\t"
      "s_add_u32 m0, s51, s54\n\t"
      "s_nop 0\n\t"
      "global_load_lds_dwordx4 v164, s[42:43]\n\t"
      "s_add_u32 s42, s42, 0x1c000\n\t"
      "s_addc_u32 s43, s43, 0\n\t"
      "s_add_u32 m0, s52, s54\n\t"
      "s_nop 0\n\t"
      "global_load_lds_dwordx4 v165, s[44:45]\n\t"
      "s_add_u32 s44, s44, 0x1c000\n\t"
      "s_addc_u32 s45, s45, 0\n\t"
      "s_mov_b32 m0, s59\n\t"
      : "+{s[40:41]}"(wb[0]), "+{s[42:43]}"(wb[1]), "+{s[44:45]}"(wb[2]), "+{s[46:47]}"(wb[3]), "={s54}"(s54c)
      : "{v163}"(voff[0]), "{v164}"(voff[1]), "{v165}"(voff[2]), "{v166}"(voff[3]), "{s50}"(lpo[0]), "{s51}"(lpo[1]), "{s52}"(lpo[2]), "{s53}"(lpo[3]), "{s57}"(has4)
      : "s59", "scc", "memory");
  if (w == 0) wait_words(ws, WS_KVF + tb * 64, 16, tag, lane);
  asm volatile("s_barrier" ::: "memory");
  f32x16 a0 = {}, a1 = {}, a2 = {}, a3 = {}, a4 = {}, a5 = {}, a6 = {};
  asm volatile(
      "s_nop 4\n\t"
      "s_mov_b32 s59, m0\n\t"
      "s_mov_b32 s55, 0\n\t"
      "global_load_dwordx4 v[130:133], v167, s[48:49] offset:0 sc1\n\t"
      "global_load_dwordx4 v[134:137], v167, s[48:49] offset:1024 sc1\n\t"
      "s_add_u32 s48, s48, 2048\n\t"
      "s_addc_u32 s49, s49, 0\n\t"
      "global_load_dwordx4 v[138:141], v167, s[48:49] offset:0 sc1\n\t"
      "global_load_dwordx4 v[142:145], v167, s[48:49] offset:1024 sc1\n\t"
      "s_add_u32 s48, s48, 2048\n\t"
      "s_addc_u32 s49, s49, 0\n\t"
      "global_load_dwordx4 v[146:149], v167, s[48:49] offset:0 sc1\n\t"
      "global_load_dwordx4 v[150:153], v167, s[48:49] offset:1024 sc1\n\t"
      "s_add_u32 s48, s48, 2048\n\t"
      "s_addc_u32 s49, s49, 0\n\t"
      "s_waitcnt vmcnt(4)\n\t"
      "s_barrier\n\t"
      "v_mov_b32 v162, v168\n\t"
      "ds_read_b64_tr_b16 v[114:115], v162 offset:0\n\t"
      "ds_read_b64_tr_b16 v[116:117], v162 offset:896\n\t"
      "ds_read_b64_tr_b16 v[118:119], v162 offset:32\n\t"
      "ds_read_b64_tr_b16 v[120:121], v162 offset:928\n\t"
      "ds_read_b64_tr_b16 v[122:123], v162 offset:64\n\t"
      "ds_read_b64_tr_b16 v[124:125], v162 offset:960\n\t"
      "s_waitcnt lgkmcnt(4)\n\t"
      "v_mfma_f32_32x32x16_bf16 v[2:17], v[114:117], v[130:133], v[2:17]\n\t"
      "ds_read_b64_tr_b16 v[126:127], v162 offset:96\n\t"
      "ds_read_b64_tr_b16 v[128:129], v162 offset:992\n\t"
      "s_cmp_eq_u32 s57, 0\n\t"
      "s_cbranch_scc1 GSK4_%=\n\t"
      "s_add_u32 m0, s53, s54\n\t"
      "s_nop 0\n\t"
      "global_load_lds_dwordx4 v166, s[46:47]\n\t"
      "s_add_u32 s46, s46, 0x1c000\n\t"
      "s_addc_u32 s47, s47, 0\n\t"
      "GSK4_%=:\n\t"
      "s_waitcnt lgkmcnt(4)\n\t"
      "v_mfma_f32_32x32x16_bf16 v[18:33], v[118:121], v[130:133], v[18:33]\n\t"
      "ds_read_b64_tr_b16 v[114:115], v162 offset:128\n\t"
      "ds_read_b64_tr_b16 v[116:117], v162 offset:1024\n\t"
      "global_load_dwordx4 v[154:157], v167, s[48:49] offset:0 sc1\n\t"
      "s_waitcnt lgkmcnt(4)\n\t"
      "v_mfma_f32_32x32x16_bf16 v[34:49], v[122:125], v[130:133], v[34:49]\n\t"
      "ds_read_b64_tr_b16 v[118:119], v162 offset:160\n\t"
      "ds_read_b64_tr_b16 v[120:121], v162 offset:1056\n\t"
      "global_load_dwordx4 v[158:161], v167, s[48:49] offset:1024 sc1\n\t"
      "s_add_u32 s48, s48, 2048\n\t"
      "s_addc_u32 s49, s49, 0\n\t"
      "s_add_u32 s54, s54, 14464\n\t"
      "s_cmp_eq_u32 s54, 72320\n\t"
      "s_cselect_b32 s54, 0, s54\n\t"
      "s_waitcnt lgkmcnt(4)\n\t"
      "v_mfma_f32_32x32x16_bf16 v[50:65], v[126:129], v[130:133], v[50:65]\n\t"
      "ds_read_b64_tr_b16 v[122:123], v162 offset:192\n\t"
      "ds_read_b64_tr_b16 v[124:125], v162 offset:1088\n\t"
      "s_waitcnt lgkmcnt(4)\n\t"
      "v_mfma_f32_32x32x16_bf16 v[66:81], v[114:117], v[130:133], v[66:81]\n\t"
      "ds_read_b64_tr_b16 v[126:127], v162 offset:3584\n\t"
      "ds_read_b64_tr_b16 v[128:129], v162 offset:4480\n\t"
      "s_waitcnt lgkmcnt(4)\n\t"
      "v_mfma_f32_32x32x16_bf16 v[82:97], v[118:121], v[130:133], v[82:97]\n\t"
      "ds_read_b64_tr_b16 v[114:115], v162 offset:3616\n\t"
      "ds_read_b64_tr_b16 v[116:117], v162 offset:4512\n\t"
      "s_waitcnt lgkmcnt(4)\n\t"
      "v_mfma_f32_32x32x16_bf16 v[98:113], v[122:125], v[130:133], v[98:113]\n\t"
      "ds_read_b64_tr_b16 v[118:119], v162 offset:3648\n\t"
      "ds_read_b64_tr_b16 v[120:121], v162 offset:4544\n\t"
      "s_waitcnt lgkmcnt(4)\n\t"
      "v_mfma_f32_32x32x16_bf16 v[2:17], v[126:129], v[134:137], v[2:17]\n\t"
      "ds_read_b64_tr_b16 v[122:123], v162 offset:3680\n\t"
      "ds_read_b64_tr_b16 v[124:125], v162 offset:4576\n\t"
      "s_waitcnt lgkmcnt(4)\n\t"
      "v_mfma_f32_32x32x16_bf16 v[18:33], v[114:117], v[134:137], v[18:33]\n\t"
      "ds_read_b64_tr_b16 v[126:127], v162 offset:3712\n\t"
      "ds_read_b64_tr_b16 v[128:129], v162 offset:4608\n\t"
      "s_waitcnt lgkmcnt(4)\n\t"
      "v_mfma_f32_32x32x16_bf16 v[34:49], v[118:121], v[134:137], v[34:49]\n\t"
      "ds_read_b64_tr_b16 v[114:115], v162 offset:3744\n\t"
      "ds_read_b64_tr_b16 v[116:117], v162 offset:4640\n\t"
      "s_waitcnt lgkmcnt(4)\n\t"
      "v_mfma_f32_32x32x16_bf16 v[50:65], v[122:125], v[134:137], v[50:65]\n\t"
      "ds_read_b64_tr_b16 v[118:119], v162 offset:3776\n\t"
      "ds_read_b64_tr_b16 v[120:121], v162 offset:4672\n\t"
      "s_waitcnt lgkmcnt(4)\n\t"
      "v_mfma_f32_32x32x16_bf16 v[66:81], v[126:129], v[134:137], v[66:81]\n\t"
      "s_waitcnt vmcnt(4)\n\t"
      "s_barrier\n\t"
      "s_add_u32 s55, s55, 14464\n\t"
      "s_cmp_eq_u32 s55, 72320\n\t"
      "s_cselect_b32 s55, 0, s55\n\t"
      "v_add_u32 v162, s55, v168\n\t"
      "ds_read_b64_tr_b16 v[122:123], v162 offset:0\n\t"
      "ds_read_b64_tr_b16 v[124:125], v162 offset:896\n\t"
      "s_add_u32 m0, s50, s54\n\t"
      "s_nop 0\n\t"
      "global_load_lds_dwordx4 v163, s[40:41]\n\t"
      "s_add_u32 s40, s40, 0x1c000\n\t"
      "s_addc_u32 s41, s41, 0\n\t"
      "s_add_u32 m0, s51, s54\n\t"
      "s_waitcnt lgkmcnt(4)\n\t"
      "v_mfma_f32_32x32x16_bf16 v[82:97], v[114:117], v[134:137], v[82:97]\n\t"
      "ds_read_b64_tr_b16 v[126:127], v162 offset:32\n\t"
      "ds_read_b64_tr_b16 v[128:129], v162 offset:928\n\t"
      "global_load_lds_dwordx4 v164, s[42:43]\n\t"
      "s_add_u32 s42, s42, 0x1c000\n\t"
      "s_addc_u32 s43, s43, 0\n\t"
      "s_add_u32 m0, s52, s54\n\t"
      "s_waitcnt lgkmcnt(4)\n\t"
      "v_mfma_f32_32x32x16_bf16 v[98:113], v[118:121], v[134:137], v[98:113]\n\t"
      "ds_read_b64_tr_b16 v[114:115], v162 offset:64\n\t"
      "ds_read_b64_tr_b16 v[116:117], v162 offset:960\n\t"
      "global_load_lds_dwordx4 v165, s[44:45]\n\t"
      "s_add_u32 s44, s44, 0x1c000\n\t"
      "s_addc_u32 s45, s45, 0\n\t"
      "s_waitcnt lgkmcnt(4)\n\t"
      "v_mfma_f32_32x32x16_bf16 v[2:17], v[122:125], v[138:141], v[2:17]\n\t"
      "ds_read_b64_tr_b16 v[118:119], v162 offset:96\n\t"
      "ds_read_b64_tr_b16 v[120:121], v162 offset:992\n\t"
      "s_cmp_eq_u32 s57, 0\n\t"
      "s_cbranch_scc1 GSK5_%=\n\t"
      "s_add_u32 m0, s53, s54\n\t"
      "s_nop 0\n\t"
      "global_load_lds_dwordx4 v166, s[46:47]\n\t"
      "s_add_u32 s46, s46, 0x1c000\n\t"
      "s_addc_u32 s47, s47, 0\n\t"
      "GSK5_%=:\n\t"
      "s_waitcnt lgkmcnt(4)\n\t"
      "v_mfma_f32_32x32x16_bf16 v[18:33], v[126:129], v[138:141], v[18:33]\n\t"
      "ds_read_b64_tr_b16 v[122:123], v162 offset:128\n\t"
      "ds_read_b64_tr_b16 v[124:125], v162 offset:1024\n\t"
      "global_load_dwordx4 v[130:133], v167, s[48:49] offset:0 sc1\n\t"
      "s_waitcnt lgkmcnt(4)\n\t"
      "v_mfma_f32_32x32x16_bf16 v[34:49], v[114:117], v[138:141], v[34:49]\n\t"
      "ds_read_b64_tr_b16 v[126:127], v162 offset:160\n\t"
      "ds_read_b64_tr_b16 v[128:129], v162 offset:1056\n\t"
      "global_load_dwordx4 v[134:137], v167, s[48:49] offset:1024 sc1\n\t"
      "s_add_u32 s48, s48, 2048\n\t"
      "s_addc_u32 s49, s49, 0\n\t"
      "s_add_u32 s54, s54, 14464\n\t"
      "s_cmp_eq_u32 s54, 72320\n\t"
      "s_cselect_b32 s54, 0, s54\n\t"
      "s_waitcnt lgkmcnt(4)\n\t"
      "v_mfma_f32_32x32x16_bf16 v[50:65], v[118:121], v[138:141], v[50:65]\n\t"
      "ds_read_b64_tr_b16 v[114:115], v162 offset:192\n\t"
      "ds_read_b64_tr_b16 v[116:117], v162 offset:1088\n\t"
      "s_waitcnt lgkmcnt(4)\n\t"
      "v_mfma_f32_32x32x16_bf16 v[66:81], v[122:125], v[138:141], v[66:81]\n\t"
      "ds_read_b64_tr_b16 v[118:119], v162 offset:3584\n\t"
      "ds_read_b64_tr_b16 v[120:121], v162 offset:4480\n\t"
      "s_waitcnt lgkmcnt(4)\n\t"
      "v_mfma_f32_32x32x16_bf16 v[82:97], v[126:129], v[138:141], v[82:97]\n\t"
      "ds_read_b64_tr_b16 v[122:123], v162 offset:3616\n\t"
      "ds_read_b64_tr_b16 v[124:125], v162 offset:4512\n\t"
      "s_waitcnt lgkmcnt(4)\n\t"
      "v_mfma_f32_32x32x16_bf16 v[98:113], v[114:117], v[138:141], v[98:113]\n\t"
      "ds_read_b64_tr_b16 v[126:127], v162 offset:3648\n\t"
      "ds_read_b64_tr_b16 v[128:129], v162 offset:4544\n\t"
      "s_waitcnt lgkmcnt(4)\n\t"
      "v_mfma_f32_32x32x16_bf16 v[2:17], v[118:121], v[142:145], v[2:17]\n\t"
      "ds_read_b64_tr_b16 v[114:115], v162 offset:3680\n\t"
      "ds_read_b64_tr_b16 v[116:117], v162 offset:4576\n\t"
      "s_waitcnt lgkmcnt(4)\n\t"
      "v_mfma_f32_32x32x16_bf16 v[18:33], v[122:125], v[142:145], v[18:33]\n\t"
      "ds_read_b64_tr_b16 v[118:119], v162 offset:3712\n\t"
      "ds_read_b64_tr_b16 v[120:121], v162 offset:4608\n\t"
      "s_waitcnt lgkmcnt(4)\n\t"
      "v_mfma_f32_32x32x16_bf16 v[34:49], v[126:129], v[142:145], v[34:49]\n\t"
      "ds_read_b64_tr_b16 v[122:123], v162 offset:3744\n\t"
      "ds_read_b64_tr_b16 v[124:125], v162 offset:4640\n\t"
      "s_waitcnt lgkmcnt(4)\n\t"
      "v_mfma_f32_32x32x16_bf16 v[50:65], v[114:117], v[142:145], v[50:65]\n\t"
      "ds_read_b64_tr_b16 v[126:127], v162 offset:3776\n\t"
      "ds_read_b64_tr_b16 v[128:129], v162 offset:4672\n\t"
      "s_waitcnt lgkmcnt(4)\n\t"
      "v_mfma_f32_32x32x16_bf16 v[66:81], v[118:121], v[142:145], v[66:81]\n\t"
      "s_waitcnt vmcnt(7)\n\t"
      "s_barrier\n\t"
      "s_add_u32 s55, s55, 14464\n\t"
      "s_cmp_eq_u32 s55, 72320\n\t"
      "s_cselect_b32 s55, 0, s55\n\t"
      "v_add_u32 v162, s55, v168\n\t"
      "ds_read_b64_tr_b16 v[114:115], v162 offset:0\n\t"
      "ds_read_b64_tr_b16 v[116:117], v162 offset:896\n\t"
      "s_add_u32 m0, s50, s54\n\t"
      "s_nop 0\n\t"
      "global_load_lds_dwordx4 v163, s[40:41]\n\t"
      "s_add_u32 s40, s40, 0x1c000\n\t"
      "s_addc_u32 s41, s41, 0\n\t"
      "s_add_u32 m0, s51, s54\n\t"
      "s_waitcnt lgkmcnt(4)\n\t"
      "v_mfma_f32_32x32x16_bf16 v[82:97], v[122:125], v[142:145], v[82:97]\n\t"
      "ds_read_b64_tr_b16 v[118:119], v162 offset:32\n\t"
      "ds_read_b64_tr_b16 v[120:121], v162 offset:928\n\t"
      "global_load_lds_dwordx4 v164, s[42:43]\n\t"
      "s_add_u32 s42, s42, 0x1c000\n\t"
      "s_addc_u32 s43, s43, 0\n\t"
      "s_add_u32 m0, s52, s54\n\t"
      "s_waitcnt lgkmcnt(4)\n\t"
      "v_mfma_f32_32x32x16_bf16 v[98:113], v[126:129], v[142:145], v[98:113]\n\t"
      "ds_read_b64_tr_b16 v[122:123], v162 offset:64\n\t"
      "ds_read_b64_tr_b16 v[124:125], v162 offset:960\n\t"
      "global_load_lds_dwordx4 v165, s[44:45]\n\t"
      "s_add_u32 s44, s44, 0x1c000\n\t"
      "s_addc_u32 s45, s45, 0\n\t"
      "s_waitcnt lgkmcnt(4)\n\t"
      "v_mfma_f32_32x32x16_bf16 v[2:17], v[114:117], v[146:149], v[2:17]\n\t"
      "ds_read_b64_tr_b16 v[126:127], v162 offset:96\n\t"
      "ds_read_b64_tr_b16 v[128:129], v162 offset:992\n\t"
      "s_cmp_eq_u32 s57, 0\n\t"
      "s_cbranch_scc1 GSK6_%=\n\t"
      "s_add_u32 m0, s53, s54\n\t"
      "s_nop 0\n\t"
      "global_load_lds_dwordx4 v166, s[46:47]\n\t"
      "s_add_u32 s46, s46, 0x1c000\n\t"
      "s_addc_u32 s47, s47, 0\n\t"
      "GSK6_%=:\n\t"
      "s_waitcnt lgkmcnt(4)\n\t"
      "v_mfma_f32_32x32x16_bf16 v[18:33], v[118:121], v[146:149], v[18:33]\n\t"
      "ds_read_b64_tr_b16 v[114:115], v162 offset:128\n\t"
      "ds_read_b64_tr_b16 v[116:117], v162 offset:1024\n\t"
      "global_load_dwordx4 v[138:141], v167, s[48:49] offset:0 sc1\n\t"
      "s_waitcnt lgkmcnt(4)\n\t"
      "v_mfma_f32_32x32x16_bf16 v[34:49], v[122:125], v[146:149], v[34:49]\n\t"
      "ds_read_b64_tr_b16 v[118:119], v162 offset:160\n\t"
      "ds_read_b64_tr_b16 v[120:121], v162 offset:1056\n\t"
      "global_load_dwordx4 v[142:145], v167, s[48:49] offset:1024 sc1\n\t"
      "s_add_u32 s48, s48, 2048\n\t"
      "s_addc_u32 s49, s49, 0\n\t"
      "s_add_u32 s54, s54, 14464\n\t"
      "s_cmp_eq_u32 s54, 72320\n\t"
      "s_cselect_b32 s54, 0, s54\n\t"
      "s_waitcnt lgkmcnt(4)\n\t"
      "v_mfma_f32_32x32x16_bf16 v[50:65], v[126:129], v[146:149], v[50:65]\n\t"
      "ds_read_b64_tr_b16 v[122:123], v162 offset:192\n\t"
      "ds_read_b64_tr_b16 v[124:125], v162 offset:1088\n\t"
      "s_waitcnt lgkmcnt(4)\n\t"
      "v_mfma_f32_32x32x16_bf16 v[66:81], v[114:117], v[146:149], v[66:81]\n\t"
      "ds_read_b64_tr_b16 v[126:127], v162 offset:3584\n\t"
      "ds_read_b64_tr_b16 v[128:129], v162 offset:4480\n\t"
      "s_waitcnt lgkmcnt(4)\n\t"
      "v_mfma_f32_32x32x16_bf16 v[82:97], v[118:121], v[146:149], v[82:97]\n\t"
      "ds_read_b64_tr_b16 v[114:115], v162 offset:3616\n\t"
      "ds_read_b64_tr_b16 v[116:117], v162 offset:4512\n\t"
      "s_waitcnt lgkmcnt(4)\n\t"
      "v_mfma_f32_32x32x16_bf16 v[98:113], v[122:125], v[146:149], v[98:113]\n\t"
      "ds_read_b64_tr_b16 v[118:119], v162 offset:3648\n\t"
      "ds_read_b64_tr_b16 v[120:121], v162 offset:4544\n\t"
      "s_waitcnt lgkmcnt(4)\n\t"
      "v_mfma_f32_32x32x16_bf16 v[2:17], v[126:129], v[150:153], v[2:17]\n\t"
      "ds_read_b64_tr_b16 v[122:123], v162 offset:3680\n\t"
      "ds_read_b64_tr_b16 v[124:125], v162 offset:4576\n\t"
      "s_waitcnt lgkmcnt(4)\n\t"
      "v_mfma_f32_32x32x16_bf16 v[18:33], v[114:117], v[150:153], v[18:33]\n\t"
      "ds_read_b64_tr_b16 v[126:127], v162 offset:3712\n\t"
      "ds_read_b64_tr_b16 v[128:129], v162 offset:4608\n\t"
      "s_waitcnt lgkmcnt(4)\n\t"
      "v_mfma_f32_32x32x16_bf16 v[34:49], v[118:121], v[150:153], v[34:49]\n\t"
      "ds_read_b64_tr_b16 v[114:115], v162 offset:3744\n\t"
      "ds_read_b64_tr_b16 v[116:117], v162 offset:4640\n\t"
      "s_waitcnt lgkmcnt(4)\n\t"
      "v_mfma_f32_32x32x16_bf16 v[50:65], v[122:125], v[150:153], v[50:65]\n\t"
      "ds_read_b64_tr_b16 v[118:119], v162 offset:3776\n\t"
      "ds_read_b64_tr_b16 v[120:121], v162 offset:4672\n\t"
      "s_waitcnt lgkmcnt(4)\n\t"
      "v_mfma_f32_32x32x16_bf16 v[66:81], v[126:129], v[150:153], v[66:81]\n\t"
      "s_waitcnt vmcnt(10)\n\t"
      "s_barrier\n\t"
      "s_add_u32 s55, s55, 14464\n\t"
      "s_cmp_eq_u32 s55, 72320\n\t"
      "s_cselect_b32 s55, 0, s55\n\t"
      "v_add_u32 v162, s55, v168\n\t"
      "ds_read_b64_tr_b16 v[122:123], v162 offset:0\n\t"
      "ds_read_b64_tr_b16 v[124:125], v162 offset:896\n\t"
      "s_add_u32 m0, s50, s54\n\t"
      "s_nop 0\n\t"
      "global_load_lds_dwordx4 v163, s[40:41]\n\t"
      "s_add_u32 s40, s40, 0x1c000\n\t"
      "s_addc_u32 s41, s41, 0\n\t"
      "s_add_u32 m0, s51, s54\n\t"
      "s_waitcnt lgkmcnt(4)\n\t"
      "v_mfma_f32_32x32x16_bf16 v[82:97], v[114:117], v[150:153], v[82:97]\n\t"
      "ds_read_b64_tr_b16 v[126:127], v162 offset:32\n\t"
      "ds_read_b64_tr_b16 v[128:129], v162 offset:928\n\t"
      "global_load_lds_dwordx4 v164, s[42:43]\n\t"
      "s_add_u32 s42, s42, 0x1c000\n\t"
      "s_addc_u32 s43, s43, 0\n\t"
      "s_add_u32 m0, s52, s54\n\t"
      "s_waitcnt lgkmcnt(4)\n\t"
      "v_mfma_f32_32x32x16_bf16 v[98:113], v[118:121], v[150:153], v[98:113]\n\t"
      "ds_read_b64_tr_b16 v[114:115], v162 offset:64\n\t"
      "ds_read_b64_tr_b16 v[116:117], v162 offset:960\n\t"
      "global_load_lds_dwordx4 v165, s[44:45]\n\t"
      "s_add_u32 s44, s44, 0x1c000\n\t"
      "s_addc_u32 s45, s45, 0\n\t"
      "s_waitcnt lgkmcnt(4)\n\t"
      "v_mfma_f32_32x32x16_bf16 v[2:17], v[122:125], v[154:157], v[2:17]\n\t"
      "ds_read_b64_tr_b16 v[118:119], v162 offset:96\n\t"
      "ds_read_b64_tr_b16 v[120:121], v162 offset:992\n\t"
      "s_cmp_eq_u32 s57, 0\n\t"
      "s_cbranch_scc1 GSK7_%=\n\t"
      "s_add_u32 m0, s53, s54\n\t"
      "s_nop 0\n\t"
      "global_load_lds_dwordx4 v166, s[46:47]\n\t"
      "s_add_u32 s46, s46, 0x1c000\n\t"
      "s_addc_u32 s47, s47, 0\n\t"
      "GSK7_%=:\n\t"
      "s_waitcnt lgkmcnt(4)\n\t"
      "v_mfma_f32_32x32x16_bf16 v[18:33], v[126:129], v[154:157], v[18:33]\n\t"
      "ds_read_b64_tr_b16 v[122:123], v162 offset:128\n\t"
      "ds_read_b64_tr_b16 v[124:125], v162 offset:1024\n\t"
      "global_load_dwordx4 v[146:149], v167, s[48:49] offset:0 sc1\n\t"
      "s_waitcnt lgkmcnt(4)\n\t"
      "v_mfma_f32_32x32x16_bf16 v[34:49], v[114:117], v[154:157], v[34:49]\n\t"
      "ds_read_b64_tr_b16 v[126:127], v162 offset:160\n\t"
      "ds_read_b64_tr_b16 v[128:129], v162 offset:1056\n\t"
      "global_load_dwordx4 v[150:153], v167, s[48:49] offset:1024 sc1\n\t"
      "s_add_u32 s48, s48, 2048\n\t"
      "s_addc_u32 s49, s49, 0\n\t"
      "s_add_u32 s54, s54, 14464\n\t"
      "s_cmp_eq_u32 s54, 72320\n\t"
      "s_cselect_b32 s54, 0, s54\n\t"
      "s_waitcnt lgkmcnt(4)\n\t"
      "v_mfma_f32_32x32x16_bf16 v[50:65], v[118:121], v[154:157], v[50:65]\n\t"
      "ds_read_b64_tr_b16 v[114:115], v162 offset:192\n\t"
      "ds_read_b64_tr_b16 v[116:117], v162 offset:1088\n\t"
      "s_waitcnt lgkmcnt(4)\n\t"
      "v_mfma_f32_32x32x16_bf16 v[66:81], v[122:125], v[154:157], v[66:81]\n\t"
      "ds_read_b64_tr_b16 v[118:119], v162 offset:3584\n\t"
      "ds_read_b64_tr_b16 v[120:121], v162 offset:4480\n\t"
      "s_waitcnt lgkmcnt(4)\n\t"
      "v_mfma_f32_32x32x16_bf16 v[82:97], v[126:129], v[154:157], v[82:97]\n\t"
      "ds_read_b64_tr_b16 v[122:123], v162 offset:3616\n\t"
      "ds_read_b64_tr_b16 v[124:125], v162 offset:4512\n\t"
      "s_waitcnt lgkmcnt(4)\n\t"
      "v_mfma_f32_32x32x16_bf16 v[98:113], v[114:117], v[154:157], v[98:113]\n\t"
      "ds_read_b64_tr_b16 v[126:127], v162 offset:3648\n\t"
      "ds_read_b64_tr_b16 v[128:129], v162 offset:4544\n\t"
      "s_waitcnt lgkmcnt(4)\n\t"
      "v_mfma_f32_32x32x16_bf16 v[2:17], v[118:121], v[158:161], v[2:17]\n\t"
      "ds_read_b64_tr_b16 v[114:115], v162 offset:3680\n\t"
      "ds_read_b64_tr_b16 v[116:117], v162 offset:4576\n\t"
      "s_waitcnt lgkmcnt(4)\n\t"
      "v_mfma_f32_32x32x16_bf16 v[18:33], v[122:125], v[158:161], v[18:33]\n\t"
      "ds_read_b64_tr_b16 v[118:119], v162 offset:3712\n\t"
      "ds_read_b64_tr_b16 v[120:121], v162 offset:4608\n\t"
      "s_waitcnt lgkmcnt(4)\n\t"
      "v_mfma_f32_32x32x16_bf16 v[34:49], v[126:129], v[158:161], v[34:49]\n\t"
      "ds_read_b64_tr_b16 v[122:123], v162 offset:3744\n\t"
      "ds_read_b64_tr_b16 v[124:125], v162 offset:4640\n\t"
      "s_waitcnt lgkmcnt(4)\n\t"
      "v_mfma_f32_32x32x16_bf16 v[50:65], v[114:117], v[158:161], v[50:65]\n\t"
      "ds_read_b64_tr_b16 v[126:127], v162 offset:3776\n\t"
      "ds_read_b64_tr_b16 v[128:129], v162 offset:4672\n\t"
      "s_waitcnt lgkmcnt(4)\n\t"
      "v_mfma_f32_32x32x16_bf16 v[66:81], v[118:121], v[158:161], v[66:81]\n\t"
      "s_waitcnt vmcnt(10)\n\t"
      "s_barrier\n\t"
      "s_add_u32 s55, s55, 14464\n\t"
      "s_cmp_eq_u32 s55, 72320\n\t"
      "s_cselect_b32 s55, 0, s55\n\t"
      "v_add_u32 v162, s55, v168\n\t"
      "ds_read_b64_tr_b16 v[114:115], v162 offset:0\n\t"
      "ds_read_b64_tr_b16 v[116:117], v162 offset:896\n\t"
      "s_add_u32 m0, s50, s54\n\t"
      "s_nop 0\n\t"
      "global_load_lds_dwordx4 v163, s[40:41]\n\t"
      "s_add_u32 s40, s40, 0x1c000\n\t"
      "s_addc_u32 s41, s41, 0\n\t"
      "s_add_u32 m0, s51, s54\n\t"
      "s_waitcnt lgkmcnt(4)\n\t"
      "v_mfma_f32_32x32x16_bf16 v[82:97], v[122:125], v[158:161], v[82:97]\n\t"
      "ds_read_b64_tr_b16 v[118:119], v162 offset:32\n\t"
      "ds_read_b64_tr_b16 v[120:121], v162 offset:928\n\t"
      "global_load_lds_dwordx4 v164, s[42:43]\n\t"
      "s_add_u32 s42, s42, 0x1c000\n\t"
      "s_addc_u32 s43, s43, 0\n\t"
      "s_add_u32 m0, s52, s54\n\t"
      "s_waitcnt lgkmcnt(4)\n\t"
      "v_mfma_f32_32x32x16_bf16 v[98:113], v[126:129], v[158:161], v[98:113]\n\t"
      "ds_read_b64_tr_b16 v[122:123], v162 offset:64\n\t"
      "ds_read_b64_tr_b16 v[124:125], v162 offset:960\n\t"
      "global_load_lds_dwordx4 v165, s[44:45]\n\t"
      "s_add_u32 s44, s44, 0x1c000\n\t"
      "s_addc_u32 s45, s45, 0\n\t"
      "s_mov_b32 s56, 2\n\t"
      "GLOOP%=:\n\t"
      "s_waitcnt lgkmcnt(4)\n\t"
      "v_mfma_f32_32x32x16_bf16 v[2:17], v[114:117], v[130:133], v[2:17]\n\t"
      "ds_read_b64_tr_b16 v[126:127], v162 offset:96\n\t"
      "ds_read_b64_tr_b16 v[128:129], v162 offset:992\n\t"
      "s_cmp_eq_u32 s57, 0\n\t"
      "s_cbranch_scc1 GSK8_%=\n\t"
      "s_add_u32 m0, s53, s54\n\t"
      "s_nop 0\n\t"
      "global_load_lds_dwordx4 v166, s[46:47]\n\t"
      "s_add_u32 s46, s46, 0x1c000\n\t"
      "s_addc_u32 s47, s47, 0\n\t"
      "GSK8_%=:\n\t"
      "s_waitcnt lgkmcnt(4)\n\t"
      "v_mfma_f32_32x32x16_bf16 v[18:33], v[118:121], v[130:133], v[18:33]\n\t"
      "ds_read_b64_tr_b16 v[114:115], v162 offset:128\n\t"
      "ds_read_b64_tr_b16 v[116:117], v162 offset:1024\n\t"
      "global_load_dwordx4 v[154:157], v167, s[48:49] offset:0 sc1\n\t"
      "s_waitcnt lgkmcnt(4)\n\t"
      "v_mfma_f32_32x32x16_bf16 v[34:49], v[122:125], v[130:133], v[34:49]\n\t"
      "ds_read_b64_tr_b16 v[118:119], v162 offset:160\n\t"
      "ds_read_b64_tr_b16 v[120:121], v162 offset:1056\n\t"
      "global_load_dwordx4 v[158:161], v167, s[48:49] offset:1024 sc1\n\t"
      "s_add_u32 s48, s48, 2048\n\t"
      "s_addc_u32 s49, s49, 0\n\t"
      "s_add_u32 s54, s54, 14464\n\t"
      "s_cmp_eq_u32 s54, 72320\n\t"
      "s_cselect_b32 s54, 0, s54\n\t"
      "s_waitcnt lgkmcnt(4)\n\t"
      "v_mfma_f32_32x32x16_bf16 v[50:65], v[126:129], v[130:133], v[50:65]\n\t"
      "ds_read_b64_tr_b16 v[122:123], v162 offset:192\n\t"
      "ds_read_b64_tr_b16 v[124:125], v162 offset:1088\n\t"
      "s_waitcnt lgkmcnt(4)\n\t"
      "v_mfma_f32_32x32x16_bf16 v[66:81], v[114:117], v[130:133], v[66:81]\n\t"
      "ds_read_b64_tr_b16 v[126:127], v162 offset:3584\n\t"
      "ds_read_b64_tr_b16 v[128:129], v162 offset:4480\n\t"
      "s_waitcnt lgkmcnt(4)\n\t"
      "v_mfma_f32_32x32x16_bf16 v[82:97], v[118:121], v[130:133], v[82:97]\n\t"
      "ds_read_b64_tr_b16 v[114:115], v162 offset:3616\n\t"
      "ds_read_b64_tr_b16 v[116:117], v162 offset:4512\n\t"
      "s_waitcnt lgkmcnt(4)\n\t"
      "v_mfma_f32_32x32x16_bf16 v[98:113], v[122:125], v[130:133], v[98:113]\n\t"
      "ds_read_b64_tr_b16 v[118:119], v162 offset:3648\n\t"
      "ds_read_b64_tr_b16 v[120:121], v162 offset:4544\n\t"
      "s_waitcnt lgkmcnt(4)\n\t"
      "v_mfma_f32_32x32x16_bf16 v[2:17], v[126:129], v[134:137], v[2:17]\n\t"
      "ds_read_b64_tr_b16 v[122:123], v162 offset:3680\n\t"
      "ds_read_b64_tr_b16 v[124:125], v162 offset:4576\n\t"
      "s_waitcnt lgkmcnt(4)\n\t"
      "v_mfma_f32_32x32x16_bf16 v[18:33], v[114:117], v[134:137], v[18:33]\n\t"
      "ds_read_b64_tr_b16 v[126:127], v162 offset:3712\n\t"
      "ds_read_b64_tr_b16 v[128:129], v162 offset:4608\n\t"
      "s_waitcnt lgkmcnt(4)\n\t"
      "v_mfma_f32_32x32x16_bf16 v[34:49], v[118:121], v[134:137], v[34:49]\n\t"
      "ds_read_b64_tr_b16 v[114:115], v162 offset:3744\n\t"
      "ds_read_b64_tr_b16 v[116:117], v162 offset:4640\n\t"
      "s_waitcnt lgkmcnt(4)\n\t"
      "v_mfma_f32_32x32x16_bf16 v[50:65], v[122:125], v[134:137], v[50:65]\n\t"
      "ds_read_b64_tr_b16 v[118:119], v162 offset:3776\n\t"
      "ds_read_b64_tr_b16 v[120:121], v162 offset:4672\n\t"
      "s_waitcnt lgkmcnt(4)\n\t"
      "v_mfma_f32_32x32x16_bf16 v[66:81], v[126:129], v[134:137], v[66:81]\n\t"
      "s_waitcnt vmcnt(10)\n\t"
      "s_barrier\n\t"
      "s_add_u32 s55, s55, 14464\n\t"
      "s_cmp_eq_u32 s55, 72320\n\t"
      "s_cselect_b32 s55, 0, s55\n\t"
      "v_add_u32 v162, s55, v168\n\t"
      "ds_read_b64_tr_b16 v[122:123], v162 offset:0\n\t"
      "ds_read_b64_tr_b16 v[124:125], v162 offset:896\n\t"
      "s_add_u32 m0, s50, s54\n\t"
      "s_nop 0\n\t"
      "global_load_lds_dwordx4 v163, s[40:41]\n\t"
      "s_add_u32 s40, s40, 0x1c000\n\t"
      "s_addc_u32 s41, s41, 0\n\t"
      "s_add_u32 m0, s51, s54\n\t"
      "s_waitcnt lgkmcnt(4)\n\t"
      "v_mfma_f32_32x32x16_bf16 v[82:97], v[114:117], v[134:137], v[82:97]\n\t"
      "ds_read_b64_tr_b16 v[126:127], v162 offset:32\n\t"
      "ds_read_b64_tr_b16 v[128:129], v162 offset:928\n\t"
      "global_load_lds_dwordx4 v164, s[42:43]\n\t"
      "s_add_u32 s42, s42, 0x1c000\n\t"
      "s_addc_u32 s43, s43, 0\n\t"
      "s_add_u32 m0, s52, s54\n\t"
      "s_waitcnt lgkmcnt(4)\n\t"
      "v_mfma_f32_32x32x16_bf16 v[98:113], v[118:121], v[134:137], v[98:113]\n\t"
      "ds_read_b64_tr_b16 v[114:115], v162 offset:64\n\t"
      "ds_read_b64_tr_b16 v[116:117], v162 offset:960\n\t"
      "global_load_lds_dwordx4 v165, s[44:45]\n\t"
      "s_add_u32 s44, s44, 0x1c000\n\t"
      "s_addc_u32 s45, s45, 0\n\t"
      "s_waitcnt lgkmcnt(4)\n\t"
      "v_mfma_f32_32x32x16_bf16 v[2:17], v[122:125], v[138:141], v[2:17]\n\t"
      "ds_read_b64_tr_b16 v[118:119], v162 offset:96\n\t"
      "ds_read_b64_tr_b16 v[120:121], v162 offset:992\n\t"
      "s_cmp_eq_u32 s57, 0\n\t"
      "s_cbranch_scc1 GSK9_%=\n\t"
      "s_add_u32 m0, s53, s54\n\t"
      "s_nop 0\n\t"
      "global_load_lds_dwordx4 v166, s[46:47]\n\t"
      "s_add_u32 s46, s46, 0x1c000\n\t"
      "s_addc_u32 s47, s47, 0\n\t"
      "GSK9_%=:\n\t"
      "s_waitcnt lgkmcnt(4)\n\t"
      "v_mfma_f32_32x32x16_bf16 v[18:33], v[126:129], v[138:141], v[18:33]\n\t"
      "ds_read_b64_tr_b16 v[122:123], v162 offset:128\n\t"
      "ds_read_b64_tr_b16 v[124:125], v162 offset:1024\n\t"
      "global_load_dwordx4 v[130:133], v167, s[48:49] offset:0 sc1\n\t"
      "s_waitcnt lgkmcnt(4)\n\t"
      "v_mfma_f32_32x32x16_bf16 v[34:49], v[114:117], v[138:141], v[34:49]\n\t"
      "ds_read_b64_tr_b16 v[126:127], v162 offset:160\n\t"
      "ds_read_b64_tr_b16 v[128:129], v162 offset:1056\n\t"
      "global_load_dwordx4 v[134:137], v167, s[48:49] offset:1024 sc1\n\t"
      "s_add_u32 s48, s48, 2048\n\t"
      "s_addc_u32 s49, s49, 0\n\t"
      "s_add_u32 s54, s54, 14464\n\t"
      "s_cmp_eq_u32 s54, 72320\n\t"
      "s_cselect_b32 s54, 0, s54\n\t"
      "s_waitcnt lgkmcnt(4)\n\t"
      "v_mfma_f32_32x32x16_bf16 v[50:65], v[118:121], v[138:141], v[50:65]\n\t"
      "ds_read_b64_tr_b16 v[114:115], v162 offset:192\n\t"
      "ds_read_b64_tr_b16 v[116:117], v162 offset:1088\n\t"
      "s_waitcnt lgkmcnt(4)\n\t"
      "v_mfma_f32_32x32x16_bf16 v[66:81], v[122:125], v[138:141], v[66:81]\n\t"
      "ds_read_b64_tr_b16 v[118:119], v162 offset:3584\n\t"
      "ds_read_b64_tr_b16 v[120:121], v162 offset:4480\n\t"
      "s_waitcnt lgkmcnt(4)\n\t"
      "v_mfma_f32_32x32x16_bf16 v[82:97], v[126:129], v[138:141], v[82:97]\n\t"
      "ds_read_b64_tr_b16 v[122:123], v162 offset:3616\n\t"
      "ds_read_b64_tr_b16 v[124:125], v162 offset:4512\n\t"
      "s_waitcnt lgkmcnt(4)\n\t"
      "v_mfma_f32_32x32x16_bf16 v[98:113], v[114:117], v[138:141], v[98:113]\n\t"
      "ds_read_b64_tr_b16 v[126:127], v162 offset:3648\n\t"
      "ds_read_b64_tr_b16 v[128:129], v162 offset:4544\n\t"
      "s_waitcnt lgkmcnt(4)\n\t"
      "v_mfma_f32_32x32x16_bf16 v[2:17], v[118:121], v[142:145], v[2:17]\n\t"
      "ds_read_b64_tr_b16 v[114:115], v162 offset:3680\n\t"
      "ds_read_b64_tr_b16 v[116:117], v162 offset:4576\n\t"
      "s_waitcnt lgkmcnt(4)\n\t"
      "v_mfma_f32_32x32x16_bf16 v[18:33], v[122:125], v[142:145], v[18:33]\n\t"
      "ds_read_b64_tr_b16 v[118:119], v162 offset:3712\n\t"
      "ds_read_b64_tr_b16 v[120:121], v162 offset:4608\n\t"
      "s_waitcnt lgkmcnt(4)\n\t"
      "v_mfma_f32_32x32x16_bf16 v[34:49], v[126:129], v[142:145], v[34:49]\n\t"
      "ds_read_b64_tr_b16 v[122:123], v162 offset:3744\n\t"
      "ds_read_b64_tr_b16 v[124:125], v162 offset:4640\n\t"
      "s_waitcnt lgkmcnt(4)\n\t"
      "v_mfma_f32_32x32x16_bf16 v[50:65], v[114:117], v[142:145], v[50:65]\n\t"
      "ds_read_b64_tr_b16 v[126:127], v162 offset:3776\n\t"
      "ds_read_b64_tr_b16 v[128:129], v162 offset:4672\n\t"
      "s_waitcnt lgkmcnt(4)\n\t"
      "v_mfma_f32_32x32x16_bf16 v[66:81], v[118:121], v[142:145], v[66:81]\n\t"
      "s_waitcnt vmcnt(10)\n\t"
      "s_barrier\n\t"
      "s_add_u32 s55, s55, 14464\n\t"
      "s_cmp_eq_u32 s55, 72320\n\t"
      "s_cselect_b32 s55, 0, s55\n\t"
      "v_add_u32 v162, s55, v168\n\t"
      "ds_read_b64_tr_b16 v[114:115], v162 offset:0\n\t"
      "ds_read_b64_tr_b16 v[116:117], v162 offset:896\n\t"
      "s_add_u32 m0, s50, s54\n\t"
      "s_nop 0\n\t"
      "global_load_lds_dwordx4 v163, s[40:41]\n\t"
      "s_add_u32 s40, s40, 0x1c000\n\t"
      "s_addc_u32 s41, s41, 0\n\t"
      "s_add_u32 m0, s51, s54\n\t"
      "s_waitcnt lgkmcnt(4)\n\t"
      "v_mfma_f32_32x32x16_bf16 v[82:97], v[122:125], v[142:145], v[82:97]\n\t"
      "ds_read_b64_tr_b16 v[118:119], v162 offset:32\n\t"
      "ds_read_b64_tr_b16 v[120:121], v162 offset:928\n\t"
      "global_load_lds_dwordx4 v164, s[42:43]\n\t"
      "s_add_u32 s42, s42, 0x1c000\n\t"
      "s_addc_u32 s43, s43, 0\n\t"
      "s_add_u32 m0, s52, s54\n\t"
      "s_waitcnt lgkmcnt(4)\n\t"
      "v_mfma_f32_32x32x16_bf16 v[98:113], v[126:129], v[142:145], v[98:113]\n\t"
      "ds_read_b64_tr_b16 v[122:123], v162 offset:64\n\t"
      "ds_read_b64_tr_b16 v[124:125], v162 offset:960\n\t"
      "global_load_lds_dwordx4 v165, s[44:45]\n\t"
      "s_add_u32 s44, s44, 0x1c000\n\t"
      "s_addc_u32 s45, s45, 0\n\t"
      "s_waitcnt lgkmcnt(4)\n\t"
      "v_mfma_f32_32x32x16_bf16 v[2:17], v[114:117], v[146:149], v[2:17]\n\t"
      "ds_read_b64_tr_b16 v[126:127], v162 offset:96\n\t"
      "ds_read_b64_tr_b16 v[128:129], v162 offset:992\n\t"
      "s_cmp_eq_u32 s57, 0\n\t"
      "s_cbranch_scc1 GSK10_%=\n\t"
      "s_add_u32 m0, s53, s54\n\t"
      "s_nop 0\n\t"
      "global_load_lds_dwordx4 v166, s[46:47]\n\t"
      "s_add_u32 s46, s46, 0x1c000\n\t"
      "s_addc_u32 s47, s47, 0\n\t"
      "GSK10_%=:\n\t"
      "s_waitcnt lgkmcnt(4)\n\t"
      "v_mfma_f32_32x32x16_bf16 v[18:33], v[118:121], v[146:149], v[18:33]\n\t"
      "ds_read_b64_tr_b16 v[114:115], v162 offset:128\n\t"
      "ds_read_b64_tr_b16 v[116:117], v162 offset:1024\n\t"
      "global_load_dwordx4 v[138:141], v167, s[48:49] offset:0 sc1\n\t"
      "s_waitcnt lgkmcnt(4)\n\t"
      "v_mfma_f32_32x32x16_bf16 v[34:49], v[122:125], v[146:149], v[34:49]\n\t"
      "ds_read_b64_tr_b16 v[118:119], v162 offset:160\n\t"
      "ds_read_b64_tr_b16 v[120:121], v162 offset:1056\n\t"
      "global_load_dwordx4 v[142:145], v167, s[48:49] offset:1024 sc1\n\t"
      "s_add_u32 s48, s48, 2048\n\t"
      "s_addc_u32 s49, s49, 0\n\t"
      "s_add_u32 s54, s54, 14464\n\t"
      "s_cmp_eq_u32 s54, 72320\n\t"
      "s_cselect_b32 s54, 0, s54\n\t"
      "s_waitcnt lgkmcnt(4)\n\t"
      "v_mfma_f32_32x32x16_bf16 v[50:65], v[126:129], v[146:149], v[50:65]\n\t"
      "ds_read_b64_tr_b16 v[122:123], v162 offset:192\n\t"
      "ds_read_b64_tr_b16 v[124:125], v162 offset:1088\n\t"
      "s_waitcnt lgkmcnt(4)\n\t"
      "v_mfma_f32_32x32x16_bf16 v[66:81], v[114:117], v[146:149], v[66:81]\n\t"
      "ds_read_b64_tr_b16 v[126:127], v162 offset:3584\n\t"
      "ds_read_b64_tr_b16 v[128:129], v162 offset:4480\n\t"
      "s_waitcnt lgkmcnt(4)\n\t"
      "v_mfma_f32_32x32x16_bf16 v[82:97], v[118:121], v[146:149], v[82:97]\n\t"
      "ds_read_b64_tr_b16 v[114:115], v162 offset:3616\n\t"
      "ds_read_b64_tr_b16 v[116:117], v162 offset:4512\n\t"
      "s_waitcnt lgkmcnt(4)\n\t"
      "v_mfma_f32_32x32x16_bf16 v[98:113], v[122:125], v[146:149], v[98:113]\n\t"
      "ds_read_b64_tr_b16 v[118:119], v162 offset:3648\n\t"
      "ds_read_b64_tr_b16 v[120:121], v162 offset:4544\n\t"
      "s_waitcnt lgkmcnt(4)\n\t"
      "v_mfma_f32_32x32x16_bf16 v[2:17], v[126:129], v[150:153], v[2:17]\n\t"
      "ds_read_b64_tr_b16 v[122:123], v162 offset:3680\n\t"
      "ds_read_b64_tr_b16 v[124:125], v162 offset:4576\n\t"
      "s_waitcnt lgkmcnt(4)\n\t"
      "v_mfma_f32_32x32x16_bf16 v[18:33], v[114:117], v[150:153], v[18:33]\n\t"
      "ds_read_b64_tr_b16 v[126:127], v162 offset:3712\n\t"
      "ds_read_b64_tr_b16 v[128:129], v162 offset:4608\n\t"
      "s_waitcnt lgkmcnt(4)\n\t"
      "v_mfma_f32_32x32x16_bf16 v[34:49], v[118:121], v[150:153], v[34:49]\n\t"
      "ds_read_b64_tr_b16 v[114:115], v162 offset:3744\n\t"
      "ds_read_b64_tr_b16 v[116:117], v162 offset:4640\n\t"
      "s_waitcnt lgkmcnt(4)\n\t"
      "v_mfma_f32_32x32x16_bf16 v[50:65], v[122:125], v[150:153], v[50:65]\n\t"
      "ds_read_b64_tr_b16 v[118:119], v162 offset:3776\n\t"
      "ds_read_b64_tr_b16 v[120:121], v162 offset:4672\n\t"
      "s_waitcnt lgkmcnt(4)\n\t"
      "v_mfma_f32_32x32x16_bf16 v[66:81], v[126:129], v[150:153], v[66:81]\n\t"
      "s_waitcnt vmcnt(10)\n\t"
      "s_barrier\n\t"
      "s_add_u32 s55, s55, 14464\n\t"
      "s_cmp_eq_u32 s55, 72320\n\t"
      "s_cselect_b32 s55, 0, s55\n\t"
      "v_add_u32 v162, s55, v168\n\t"
      "ds_read_b64_tr_b16 v[122:123], v162 offset:0\n\t"
      "ds_read_b64_tr_b16 v[124:125], v162 offset:896\n\t"
      "s_add_u32 m0, s50, s54\n\t"
      "s_nop 0\n\t"
      "global_load_lds_dwordx4 v163, s[40:41]\n\t"
      "s_add_u32 s40, s40, 0x1c000\n\t"
      "s_addc_u32 s41, s41, 0\n\t"
      "s_add_u32 m0, s51, s54\n\t"
      "s_waitcnt lgkmcnt(4)\n\t"
      "v_mfma_f32_32x32x16_bf16 v[82:97], v[114:117], v[150:153], v[82:97]\n\t"
      "ds_read_b64_tr_b16 v[126:127], v162 offset:32\n\t"
      "ds_read_b64_tr_b16 v[128:129], v162 offset:928\n\t"
      "global_load_lds_dwordx4 v164, s[42:43]\n\t"
      "s_add_u32 s42, s42, 0x1c000\n\t"
      "s_addc_u32 s43, s43, 0\n\t"
      "s_add_u32 m0, s52, s54\n\t"
      "s_waitcnt lgkmcnt(4)\n\t"
      "v_mfma_f32_32x32x16_bf16 v[98:113], v[118:121], v[150:153], v[98:113]\n\t"
      "ds_read_b64_tr_b16 v[114:115], v162 offset:64\n\t"
      "ds_read_b64_tr_b16 v[116:117], v162 offset:960\n\t"
      "global_load_lds_dwordx4 v165, s[44:45]\n\t"
      "s_add_u32 s44, s44, 0x1c000\n\t"
      "s_addc_u32 s45, s45, 0\n\t"
      "s_waitcnt lgkmcnt(4)\n\t"
      "v_mfma_f32_32x32x16_bf16 v[2:17], v[122:125], v[154:157], v[2:17]\n\t"
      "ds_read_b64_tr_b16 v[118:119], v162 offset:96\n\t"
      "ds_read_b64_tr_b16 v[120:121], v162 offset:992\n\t"
      "s_cmp_eq_u32 s57, 0\n\t"
      "s_cbranch_scc1 GSK11_%=\n\t"
      "s_add_u32 m0, s53, s54\n\t"
      "s_nop 0\n\t"
      "global_load_lds_dwordx4 v166, s[46:47]\n\t"
      "s_add_u32 s46, s46, 0x1c000\n\t"
      "s_addc_u32 s47, s47, 0\n\t"
      "GSK11_%=:\n\t"
      "s_waitcnt lgkmcnt(4)\n\t"
      "v_mfma_f32_32x32x16_bf16 v[18:33], v[126:129], v[154:157], v[18:33]\n\t"
      "ds_read_b64_tr_b16 v[122:123], v162 offset:128\n\t"
      "ds_read_b64_tr_b16 v[124:125], v162 offset:1024\n\t"
      "global_load_dwordx4 v[146:149], v167, s[48:49] offset:0 sc1\n\t"
      "s_waitcnt lgkmcnt(4)\n\t"
      "v_mfma_f32_32x32x16_bf16 v[34:49], v[114:117], v[154:157], v[34:49]\n\t"
      "ds_read_b64_tr_b16 v[126:127], v162 offset:160\n\t"
      "ds_read_b64_tr_b16 v[128:129], v162 offset:1056\n\t"
      "global_load_dwordx4 v[150:153], v167, s[48:49] offset:1024 sc1\n\t"
      "s_add_u32 s48, s48, 2048\n\t"
      "s_addc_u32 s49, s49, 0\n\t"
      "s_add_u32 s54, s54, 14464\n\t"
      "s_cmp_eq_u32 s54, 72320\n\t"
      "s_cselect_b32 s54, 0, s54\n\t"
      "s_waitcnt lgkmcnt(4)\n\t"
      "v_mfma_f32_32x32x16_bf16 v[50:65], v[118:121], v[154:157], v[50:65]\n\t"
      "ds_read_b64_tr_b16 v[114:115], v162 offset:192\n\t"
      "ds_read_b64_tr_b16 v[116:117], v162 offset:1088\n\t"
      "s_waitcnt lgkmcnt(4)\n\t"
      "v_mfma_f32_32x32x16_bf16 v[66:81], v[122:125], v[154:157], v[66:81]\n\t"
      "ds_read_b64_tr_b16 v[118:119], v162 offset:3584\n\t"
      "ds_read_b64_tr_b16 v[120:121], v162 offset:4480\n\t"
      "s_waitcnt lgkmcnt(4)\n\t"
      "v_mfma_f32_32x32x16_bf16 v[82:97], v[126:129], v[154:157], v[82:97]\n\t"
      "ds_read_b64_tr_b16 v[122:123], v162 offset:3616\n\t"
      "ds_read_b64_tr_b16 v[124:125], v162 offset:4512\n\t"
      "s_waitcnt lgkmcnt(4)\n\t"
      "v_mfma_f32_32x32x16_bf16 v[98:113], v[114:117], v[154:157], v[98:113]\n\t"
      "ds_read_b64_tr_b16 v[126:127], v162 offset:3648\n\t"
      "ds_read_b64_tr_b16 v[128:129], v162 offset:4544\n\t"
      "s_waitcnt lgkmcnt(4)\n\t"
      "v_mfma_f32_32x32x16_bf16 v[2:17], v[118:121], v[158:161], v[2:17]\n\t"
      "ds_read_b64_tr_b16 v[114:115], v162 offset:3680\n\t"
      "ds_read_b64_tr_b16 v[116:117], v162 offset:4576\n\t"
      "s_waitcnt lgkmcnt(4)\n\t"
      "v_mfma_f32_32x32x16_bf16 v[18:33], v[122:125], v[158:161], v[18:33]\n\t"
      "ds_read_b64_tr_b16 v[118:119], v162 offset:3712\n\t"
      "ds_read_b64_tr_b16 v[120:121], v162 offset:4608\n\t"
      "s_waitcnt lgkmcnt(4)\n\t"
      "v_mfma_f32_32x32x16_bf16 v[34:49], v[126:129], v[158:161], v[34:49]\n\t"
      "ds_read_b64_tr_b16 v[122:123], v162 offset:3744\n\t"
      "ds_read_b64_tr_b16 v[124:125], v162 offset:4640\n\t"
      "s_waitcnt lgkmcnt(4)\n\t"
      "v_mfma_f32_32x32x16_bf16 v[50:65], v[114:117], v[158:161], v[50:65]\n\t"
      "ds_read_b64_tr_b16 v[126:127], v162 offset:3776\n\t"
      "ds_read_b64_tr_b16 v[128:129], v162 offset:4672\n\t"
      "s_waitcnt lgkmcnt(4)\n\t"
      "v_mfma_f32_32x32x16_bf16 v[66:81], v[118:121], v[158:161], v[66:81]\n\t"
      "s_waitcnt vmcnt(10)\n\t"
      "s_barrier\n\t"
      "s_add_u32 s55, s55, 14464\n\t"
      "s_cmp_eq_u32 s55, 72320\n\t"
      "s_cselect_b32 s55, 0, s55\n\t"
      "v_add_u32 v162, s55, v168\n\t"
      "ds_read_b64_tr_b16 v[114:115], v162 offset:0\n\t"
      "ds_read_b64_tr_b16 v[116:117], v162 offset:896\n\t"
      "s_add_u32 m0, s50, s54\n\t"
      "s_nop 0\n\t"
      "global_load_lds_dwordx4 v163, s[40:41]\n\t"
      "s_add_u32 s40, s40, 0x1c000\n\t"
      "s_addc_u32 s41, s41, 0\n\t"
      "s_add_u32 m0, s51, s54\n\t"
      "s_waitcnt lgkmcnt(4)\n\t"
      "v_mfma_f32_32x32x16_bf16 v[82:97], v[122:125], v[158:161], v[82:97]\n\t"
      "ds_read_b64_tr_b16 v[118:119], v162 offset:32\n\t"
      "ds_read_b64_tr_b16 v[120:121], v162 offset:928\n\t"
      "global_load_lds_dwordx4 v164, s[42:43]\n\t"
      "s_add_u32 s42, s42, 0x1c000\n\t"
      "s_addc_u32 s43, s43, 0\n\t"
      "s_add_u32 m0, s52, s54\n\t"
      "s_waitcnt lgkmcnt(4)\n\t"
      "v_mfma_f32_32x32x16_bf16 v[98:113], v[126:129], v[158:161], v[98:113]\n\t"
      "ds_read_b64_tr_b16 v[122:123], v162 offset:64\n\t"
      "ds_read_b64_tr_b16 v[124:125], v162 offset:960\n\t"
      "global_load_lds_dwordx4 v165, s[44:45]\n\t"
      "s_add_u32 s44, s44, 0x1c000\n\t"
      "s_addc_u32 s45, s45, 0\n\t"
      "s_sub_u32 s56, s56, 1\n\t"
      "s_cmp_lg_u32 s56, 0\n\t"
      "s_cbranch_scc1 GLOOP%=\n\t"
      "s_waitcnt lgkmcnt(4)\n\t"
      "v_mfma_f32_32x32x16_bf16 v[2:17], v[114:117], v[130:133], v[2:17]\n\t"
      "ds_read_b64_tr_b16 v[126:127], v162 offset:96\n\t"
      "ds_read_b64_tr_b16 v[128:129], v162 offset:992\n\t"
      "s_cmp_eq_u32 s57, 0\n\t"
      "s_cbranch_scc1 GSK16_%=\n\t"
      "s_add_u32 m0, s53, s54\n\t"
      "s_nop 0\n\t"
      "global_load_lds_dwordx4 v166, s[46:47]\n\t"
      "s_add_u32 s46, s46, 0x1c000\n\t"
      "s_addc_u32 s47, s47, 0\n\t"
      "GSK16_%=:\n\t"
      "s_waitcnt lgkmcnt(4)\n\t"
      "v_mfma_f32_32x32x16_bf16 v[18:33], v[118:121], v[130:133], v[18:33]\n\t"
      "ds_read_b64_tr_b16 v[114:115], v162 offset:128\n\t"
      "ds_read_b64_tr_b16 v[116:117], v162 offset:1024\n\t"
      "global_load_dwordx4 v[154:157], v167, s[48:49] offset:0 sc1\n\t"
      "s_waitcnt lgkmcnt(4)\n\t"
      "v_mfma_f32_32x32x16_bf16 v[34:49], v[122:125], v[130:133], v[34:49]\n\t"
      "ds_read_b64_tr_b16 v[118:119], v162 offset:160\n\t"
      "ds_read_b64_tr_b16 v[120:121], v162 offset:1056\n\t"
      "global_load_dwordx4 v[158:161], v167, s[48:49] offset:1024 sc1\n\t"
      "s_add_u32 s48, s48, 2048\n\t"
      "s_addc_u32 s49, s49, 0\n\t"
      "s_add_u32 s54, s54, 14464\n\t"
      "s_cmp_eq_u32 s54, 72320\n\t"
      "s_cselect_b32 s54, 0, s54\n\t"
      "s_waitcnt lgkmcnt(4)\n\t"
      "v_mfma_f32_32x32x16_bf16 v[50:65], v[126:129], v[130:133], v[50:65]\n\t"
      "ds_read_b64_tr_b16 v[122:123], v162 offset:192\n\t"
      "ds_read_b64_tr_b16 v[124:125], v162 offset:1088\n\t"
      "s_waitcnt lgkmcnt(4)\n\t"
      "v_mfma_f32_32x32x16_bf16 v[66:81], v[114:117], v[130:133], v[66:81]\n\t"
      "ds_read_b64_tr_b16 v[126:127], v162 offset:3584\n\t"
      "ds_read_b64_tr_b16 v[128:129], v162 offset:4480\n\t"
      "s_waitcnt lgkmcnt(4)\n\t"
      "v_mfma_f32_32x32x16_bf16 v[82:97], v[118:121], v[130:133], v[82:97]\n\t"
      "ds_read_b64_tr_b16 v[114:115], v162 offset:3616\n\t"
      "ds_read_b64_tr_b16 v[116:117], v162 offset:4512\n\t"
      "s_waitcnt lgkmcnt(4)\n\t"
      "v_mfma_f32_32x32x16_bf16 v[98:113], v[122:125], v[130:133], v[98:113]\n\t"
      "ds_read_b64_tr_b16 v[118:119], v162 offset:3648\n\t"
      "ds_read_b64_tr_b16 v[120:121], v162 offset:4544\n\t"
      "s_waitcnt lgkmcnt(4)\n\t"
      "v_mfma_f32_32x32x16_bf16 v[2:17], v[126:129], v[134:137], v[2:17]\n\t"
      "ds_read_b64_tr_b16 v[122:123], v162 offset:3680\n\t"
      "ds_read_b64_tr_b16 v[124:125], v162 offset:4576\n\t"
      "s_waitcnt lgkmcnt(4)\n\t"
      "v_mfma_f32_32x32x16_bf16 v[18:33], v[114:117], v[134:137], v[18:33]\n\t"
      "ds_read_b64_tr_b16 v[126:127], v162 offset:3712\n\t"
      "ds_read_b64_tr_b16 v[128:129], v162 offset:4608\n\t"
      "s_waitcnt lgkmcnt(4)\n\t"
      "v_mfma_f32_32x32x16_bf16 v[34:49], v[118:121], v[134:137], v[34:49]\n\t"
      "ds_read_b64_tr_b16 v[114:115], v162 offset:3744\n\t"
      "ds_read_b64_tr_b16 v[116:117], v162 offset:4640\n\t"
      "s_waitcnt lgkmcnt(4)\n\t"
      "v_mfma_f32_32x32x16_bf16 v[50:65], v[122:125], v[134:137], v[50:65]\n\t"
      "ds_read_b64_tr_b16 v[118:119], v162 offset:3776\n\t"
      "ds_read_b64_tr_b16 v[120:121], v162 offset:4672\n\t"
      "s_waitcnt lgkmcnt(4)\n\t"
      "v_mfma_f32_32x32x16_bf16 v[66:81], v[126:129], v[134:137], v[66:81]\n\t"
      "s_waitcnt vmcnt(10)\n\t"
      "s_barrier\n\t"
      "s_add_u32 s55, s55, 14464\n\t"
      "s_cmp_eq_u32 s55, 72320\n\t"
      "s_cselect_b32 s55, 0, s55\n\t"
      "v_add_u32 v162, s55, v168\n\t"
      "ds_read_b64_tr_b16 v[122:123], v162 offset:0\n\t"
      "ds_read_b64_tr_b16 v[124:125], v162 offset:896\n\t"
      "s_waitcnt lgkmcnt(4)\n\t"
      "v_mfma_f32_32x32x16_bf16 v[82:97], v[114:117], v[134:137], v[82:97]\n\t"
      "ds_read_b64_tr_b16 v[126:127], v162 offset:32\n\t"
      "ds_read_b64_tr_b16 v[128:129], v162 offset:928\n\t"
      "s_waitcnt lgkmcnt(4)\n\t"
      "v_mfma_f32_32x32x16_bf16 v[98:113], v[118:121], v[134:137], v[98:113]\n\t"
      "ds_read_b64_tr_b16 v[114:115], v162 offset:64\n\t"
      "ds_read_b64_tr_b16 v[116:117], v162 offset:960\n\t"
      "s_waitcnt lgkmcnt(4)\n\t"
      "v_mfma_f32_32x32x16_bf16 v[2:17], v[122:125], v[138:141], v[2:17]\n\t"
      "ds_read_b64_tr_b16 v[118:119], v162 offset:96\n\t"
      "ds_read_b64_tr_b16 v[120:121], v162 offset:992\n\t"
      "s_waitcnt lgkmcnt(4)\n\t"
      "v_mfma_f32_32x32x16_bf16 v[18:33], v[126:129], v[138:141], v[18:33]\n\t"
      "ds_read_b64_tr_b16 v[122:123], v162 offset:128\n\t"
      "ds_read_b64_tr_b16 v[124:125], v162 offset:1024\n\t"
      "s_waitcnt lgkmcnt(4)\n\t"
      "v_mfma_f32_32x32x16_bf16 v[34:49], v[114:117], v[138:141], v[34:49]\n\t"
      "ds_read_b64_tr_b16 v[126:127], v162 offset:160\n\t"
      "ds_read_b64_tr_b16 v[128:129], v162 offset:1056\n\t"
      "s_waitcnt lgkmcnt(4)\n\t"
      "v_mfma_f32_32x32x16_bf16 v[50:65], v[118:121], v[138:141], v[50:65]\n\t"
      "ds_read_b64_tr_b16 v[114:115], v162 offset:192\n\t"
      "ds_read_b64_tr_b16 v[116:117], v162 offset:1088\n\t"
      "s_waitcnt lgkmcnt(4)\n\t"
      "v_mfma_f32_32x32x16_bf16 v[66:81], v[122:125], v[138:141], v[66:81]\n\t"
      "ds_read_b64_tr_b16 v[118:119], v162 offset:3584\n\t"
      "ds_read_b64_tr_b16 v[120:121], v162 offset:4480\n\t"
      "s_waitcnt lgkmcnt(4)\n\t"
      "v_mfma_f32_32x32x16_bf16 v[82:97], v[126:129], v[138:141], v[82:97]\n\t"
      "ds_read_b64_tr_b16 v[122:123], v162 offset:3616\n\t"
      "ds_read_b64_tr_b16 v[124:125], v162 offset:4512\n\t"
      "s_waitcnt lgkmcnt(4)\n\t"
      "v_mfma_f32_32x32x16_bf16 v[98:113], v[114:117], v[138:141], v[98:113]\n\t"
      "ds_read_b64_tr_b16 v[126:127], v162 offset:3648\n\t"
      "ds_read_b64_tr_b16 v[128:129], v162 offset:4544\n\t"
      "s_waitcnt lgkmcnt(4)\n\t"
      "v_mfma_f32_32x32x16_bf16 v[2:17], v[118:121], v[142:145], v[2:17]\n\t"
      "ds_read_b64_tr_b16 v[114:115], v162 offset:3680\n\t"
      "ds_read_b64_tr_b16 v[116:117], v162 offset:4576\n\t"
      "s_waitcnt lgkmcnt(4)\n\t"
      "v_mfma_f32_32x32x16_bf16 v[18:33], v[122:125], v[142:145], v[18:33]\n\t"
      "ds_read_b64_tr_b16 v[118:119], v162 offset:3712\n\t"
      "ds_read_b64_tr_b16 v[120:121], v162 offset:4608\n\t"
      "s_waitcnt lgkmcnt(4)\n\t"
      "v_mfma_f32_32x32x16_bf16 v[34:49], v[126:129], v[142:145], v[34:49]\n\t"
      "ds_read_b64_tr_b16 v[122:123], v162 offset:3744\n\t"
      "ds_read_b64_tr_b16 v[124:125], v162 offset:4640\n\t"
      "s_waitcnt lgkmcnt(4)\n\t"
      "v_mfma_f32_32x32x16_bf16 v[50:65], v[114:117], v[142:145], v[50:65]\n\t"
      "ds_read_b64_tr_b16 v[126:127], v162 offset:3776\n\t"
      "ds_read_b64_tr_b16 v[128:129], v162 offset:4672\n\t"
      "s_waitcnt lgkmcnt(4)\n\t"
      "v_mfma_f32_32x32x16_bf16 v[66:81], v[118:121], v[142:145], v[66:81]\n\t"
      "s_waitcnt vmcnt(5)\n\t"
      "s_barrier\n\t"
      "s_add_u32 s55, s55, 14464\n\t"
      "s_cmp_eq_u32 s55, 72320\n\t"
      "s_cselect_b32 s55, 0, s55\n\t"
      "v_add_u32 v162, s55, v168\n\t"
      "ds_read_b64_tr_b16 v[114:115], v162 offset:0\n\t"
      "ds_read_b64_tr_b16 v[116:117], v162 offset:896\n\t"
      "s_waitcnt lgkmcnt(4)\n\t"
      "v_mfma_f32_32x32x16_bf16 v[82:97], v[122:125], v[142:145], v[82:97]\n\t"
      "ds_read_b64_tr_b16 v[118:119], v162 offset:32\n\t"
      "ds_read_b64_tr_b16 v[120:121], v162 offset:928\n\t"
      "s_waitcnt lgkmcnt(4)\n\t"
      "v_mfma_f32_32x32x16_bf16 v[98:113], v[126:129], v[142:145], v[98:113]\n\t"
      "ds_read_b64_tr_b16 v[122:123], v162 offset:64\n\t"
      "ds_read_b64_tr_b16 v[124:125], v162 offset:960\n\t"
      "s_waitcnt lgkmcnt(4)\n\t"
      "v_mfma_f32_32x32x16_bf16 v[2:17], v[114:117], v[146:149], v[2:17]\n\t"
      "ds_read_b64_tr_b16 v[126:127], v162 offset:96\n\t"
      "ds_read_b64_tr_b16 v[128:129], v162 offset:992\n\t"
      "s_waitcnt lgkmcnt(4)\n\t"
      "v_mfma_f32_32x32x16_bf16 v[18:33], v[118:121], v[146:149], v[18:33]\n\t"
      "ds_read_b64_tr_b16 v[114:115], v162 offset:128\n\t"
      "ds_read_b64_tr_b16 v[116:117], v162 offset:1024\n\t"
      "s_waitcnt lgkmcnt(4)\n\t"
      "v_mfma_f32_32x32x16_bf16 v[34:49], v[122:125], v[146:149], v[34:49]\n\t"
      "ds_read_b64_tr_b16 v[118:119], v162 offset:160\n\t"
      "ds_read_b64_tr_b16 v[120:121], v162 offset:1056\n\t"
      "s_waitcnt lgkmcnt(4)\n\t"
      "v_mfma_f32_32x32x16_bf16 v[50:65], v[126:129], v[146:149], v[50:65]\n\t"
      "ds_read_b64_tr_b16 v[122:123], v162 offset:192\n\t"
      "ds_read_b64_tr_b16 v[124:125], v162 offset:1088\n\t"
      "s_waitcnt lgkmcnt(4)\n\t"
      "v_mfma_f32_32x32x16_bf16 v[66:81], v[114:117], v[146:149], v[66:81]\n\t"
      "ds_read_b64_tr_b16 v[126:127], v162 offset:3584\n\t"
      "ds_read_b64_tr_b16 v[128:129], v162 offset:4480\n\t"
      "s_waitcnt lgkmcnt(4)\n\t"
      "v_mfma_f32_32x32x16_bf16 v[82:97], v[118:121], v[146:149], v[82:97]\n\t"
      "ds_read_b64_tr_b16 v[114:115], v162 offset:3616\n\t"
      "ds_read_b64_tr_b16 v[116:117], v162 offset:4512\n\t"
      "s_waitcnt lgkmcnt(4)\n\t"
      "v_mfma_f32_32x32x16_bf16 v[98:113], v[122:125], v[146:149], v[98:113]\n\t"
      "ds_read_b64_tr_b16 v[118:119], v162 offset:3648\n\t"
      "ds_read_b64_tr_b16 v[120:121], v162 offset:4544\n\t"
      "s_waitcnt lgkmcnt(4)\n\t"
      "v_mfma_f32_32x32x16_bf16 v[2:17], v[126:129], v[150:153], v[2:17]\n\t"
      "ds_read_b64_tr_b16 v[122:123], v162 offset:3680\n\t"
      "ds_read_b64_tr_b16 v[124:125], v162 offset:4576\n\t"
      "s_waitcnt lgkmcnt(4)\n\t"
      "v_mfma_f32_32x32x16_bf16 v[18:33], v[114:117], v[150:153], v[18:33]\n\t"
      "ds_read_b64_tr_b16 v[126:127], v162 offset:3712\n\t"
      "ds_read_b64_tr_b16 v[128:129], v162 offset:4608\n\t"
      "s_waitcnt lgkmcnt(4)\n\t"
      "v_mfma_f32_32x32x16_bf16 v[34:49], v[118:121], v[150:153], v[34:49]\n\t"
      "ds_read_b64_tr_b16 v[114:115], v162 offset:3744\n\t"
      "ds_read_b64_tr_b16 v[116:117], v162 offset:4640\n\t"
      "s_waitcnt lgkmcnt(4)\n\t"
      "v_mfma_f32_32x32x16_bf16 v[50:65], v[122:125], v[150:153], v[50:65]\n\t"
      "ds_read_b64_tr_b16 v[118:119], v162 offset:3776\n\t"
      "ds_read_b64_tr_b16 v[120:121], v162 offset:4672\n\t"
      "s_waitcnt lgkmcnt(4)\n\t"
      "v_mfma_f32_32x32x16_bf16 v[66:81], v[126:129], v[150:153], v[66:81]\n\t"
      "s_waitcnt vmcnt(0)\n\t"
      "s_barrier\n\t"
      "s_add_u32 s55, s55, 14464\n\t"
      "s_cmp_eq_u32 s55, 72320\n\t"
      "s_cselect_b32 s55, 0, s55\n\t"
      "v_add_u32 v162, s55, v168\n\t"
      "ds_read_b64_tr_b16 v[122:123], v162 offset:0\n\t"
      "ds_read_b64_tr_b16 v[124:125], v162 offset:896\n\t"
      "s_waitcnt lgkmcnt(4)\n\t"
      "v_mfma_f32_32x32x16_bf16 v[82:97], v[114:117], v[150:153], v[82:97]\n\t"
      "ds_read_b64_tr_b16 v[126:127], v162 offset:32\n\t"
      "ds_read_b64_tr_b16 v[128:129], v162 offset:928\n\t"
      "s_waitcnt lgkmcnt(4)\n\t"
      "v_mfma_f32_32x32x16_bf16 v[98:113], v[118:121], v[150:153], v[98:113]\n\t"
      "ds_read_b64_tr_b16 v[114:115], v162 offset:64\n\t"
      "ds_read_b64_tr_b16 v[116:117], v162 offset:960\n\t"
      "s_waitcnt lgkmcnt(4)\n\t"
      "v_mfma_f32_32x32x16_bf16 v[2:17], v[122:125], v[154:157], v[2:17]\n\t"
      "ds_read_b64_tr_b16 v[118:119], v162 offset:96\n\t"
      "ds_read_b64_tr_b16 v[120:121], v162 offset:992\n\t"
      "s_waitcnt lgkmcnt(4)\n\t"
      "v_mfma_f32_32x32x16_bf16 v[18:33], v[126:129], v[154:157], v[18:33]\n\t"
      "ds_read_b64_tr_b16 v[122:123], v162 offset:128\n\t"
      "ds_read_b64_tr_b16 v[124:125], v162 offset:1024\n\t"
      "s_waitcnt lgkmcnt(4)\n\t"
      "v_mfma_f32_32x32x16_bf16 v[34:49], v[114:117], v[154:157], v[34:49]\n\t"
      "ds_read_b64_tr_b16 v[126:127], v162 offset:160\n\t"
      "ds_read_b64_tr_b16 v[128:129], v162 offset:1056\n\t"
      "s_waitcnt lgkmcnt(4)\n\t"
      "v_mfma_f32_32x32x16_bf16 v[50:65], v[118:121], v[154:157], v[50:65]\n\t"
      "ds_read_b64_tr_b16 v[114:115], v162 offset:192\n\t"
      "ds_read_b64_tr_b16 v[116:117], v162 offset:1088\n\t"
      "s_waitcnt lgkmcnt(4)\n\t"
      "v_mfma_f32_32x32x16_bf16 v[66:81], v[122:125], v[154:157], v[66:81]\n\t"
      "ds_read_b64_tr_b16 v[118:119], v162 offset:3584\n\t"
      "ds_read_b64_tr_b16 v[120:121], v162 offset:4480\n\t"
      "s_waitcnt lgkmcnt(4)\n\t"
      "v_mfma_f32_32x32x16_bf16 v[82:97], v[126:129], v[154:157], v[82:97]\n\t"
      "ds_read_b64_tr_b16 v[122:123], v162 offset:3616\n\t"
      "ds_read_b64_tr_b16 v[124:125], v162 offset:4512\n\t"
      "s_waitcnt lgkmcnt(4)\n\t"
      "v_mfma_f32_32x32x16_bf16 v[98:113], v[114:117], v[154:157], v[98:113]\n\t"
      "ds_read_b64_tr_b16 v[126:127], v162 offset:3648\n\t"
      "ds_read_b64_tr_b16 v[128:129], v162 offset:4544\n\t"
      "s_waitcnt lgkmcnt(4)\n\t"
      "v_mfma_f32_32x32x16_bf16 v[2:17], v[118:121], v[158:161], v[2:17]\n\t"
      "ds_read_b64_tr_b16 v[114:115], v162 offset:3680\n\t"
      "ds_read_b64_tr_b16 v[116:117], v162 offset:4576\n\t"
      "s_waitcnt lgkmcnt(4)\n\t"
      "v_mfma_f32_32x32x16_bf16 v[18:33], v[122:125], v[158:161], v[18:33]\n\t"
      "ds_read_b64_tr_b16 v[118:119], v162 offset:3712\n\t"
      "ds_read_b64_tr_b16 v[120:121], v162 offset:4608\n\t"
      "s_waitcnt lgkmcnt(4)\n\t"
      "v_mfma_f32_32x32x16_bf16 v[34:49], v[126:129], v[158:161], v[34:49]\n\t"
      "ds_read_b64_tr_b16 v[122:123], v162 offset:3744\n\t"
      "ds_read_b64_tr_b16 v[124:125], v162 offset:4640\n\t"
      "s_waitcnt lgkmcnt(4)\n\t"
      "v_mfma_f32_32x32x16_bf16 v[50:65], v[114:117], v[158:161], v[50:65]\n\t"
      "ds_read_b64_tr_b16 v[126:127], v162 offset:3776\n\t"
      "ds_read_b64_tr_b16 v[128:129], v162 offset:4672\n\t"
      "s_waitcnt lgkmcnt(4)\n\t"
      "v_mfma_f32_32x32x16_bf16 v[66:81], v[118:121], v[158:161], v[66:81]\n\t"
      "s_waitcnt lgkmcnt(2)\n\t"
      "v_mfma_f32_32x32x16_bf16 v[82:97], v[122:125], v[158:161], v[82:97]\n\t"
      "s_waitcnt lgkmcnt(0)\n\t"
      "v_mfma_f32_32x32x16_bf16 v[98:113], v[126:129], v[158:161], v[98:113]\n\t"
      "s_mov_b32 m0, s59\n\t"
      "s_nop 7\n\t"
      "s_nop 7\n\t"
      : "+{v[2:17]}"(a0), "+{v[18:33]}"(a1), "+{v[34:49]}"(a2), "+{v[50:65]}"(a3), "+{v[66:81]}"(a4), "+{v[82:97]}"(a5), "+{v[98:113]}"(a6),
        "+{s[40:41]}"(wb[0]), "+{s[42:43]}"(wb[1]), "+{s[44:45]}"(wb[2]), "+{s[46:47]}"(wb[3]), "+{s[48:49]}"(xsrc), "+{s54}"(s54c)
      : "{v163}"(voff[0]), "{v164}"(voff[1]), "{v165}"(voff[2]), "{v166}"(voff[3]), "{v167}"(vb), "{v168}"(alane),
        "{s50}"(lpo[0]), "{s51}"(lpo[1]), "{s52}"(lpo[2]), "{s53}"(lpo[3]), "{s57}"(has4)
      : "v114", "v115", "v116", "v117", "v118", "v119", "v120", "v121", "v122", "v123", "v124", "v125", "v126", "v127", "v128", "v129", "v130", "v131", "v132", "v133", "v134", "v135", "v136", "v137", "v138", "v139", "v140", "v141", "v142", "v143", "v144", "v145", "v146", "v147", "v148", "v149", "v150", "v151", "v152", "v153", "v154", "v155", "v156", "v157", "v158", "v159", "v160", "v161", "v162", "s55", "s56", "s59", "scc", "memory");
  asm volatile("s_barrier" ::: "memory");
  __builtin_amdgcn_s_setprio(PRIO_EPI);
  const __amdgpu_buffer_rsrc_t hr = __builtin_amdgcn_make_buffer_rsrc(ws + WS_HF, 0, 64 * 112 * 1024, 0x00020000);
  const unsigned hoff = (tt * 112 + 7 * j) * 1024 + lane * 16;
#define H_TILE(A, t) do { u32x4 o_; \
    _Pragma("unroll") for (int r = 0; r < 4; ++r) o_[r] = gelu_mul2((f32x2){A[2 * r], A[2 * r + 1]}, (f32x2){A[8 + 2 * r], A[9 + 2 * r]}); \
    __builtin_amdgcn_raw_buffer_store_b128(o_, hr, hoff + (t) * 1024, 0, 16); } while (0)
  H_TILE(a0, 0); H_TILE(a1, 1); H_TILE(a2, 2); H_TILE(a3, 3); H_TILE(a4, 4); H_TILE(a5, 5); H_TILE(a6, 6);
#undef H_TILE
  asm volatile("s_waitcnt vmcnt(0)" ::: "memory");
  if (lane == 0) set_word(ws, flag_off + w * 32, tag);
  __builtin_amdgcn_s_setprio(PRIO_FFN);
}

__device__ __forceinline__ void down_role(int tb, int cb, int fh, int dg, unsigned tag, const u16* __restrict__ wd, const u16* __restrict__ wo, const u16* __restrict__ x, u16* __restrict__ out, char* __restrict__ ws, char* lds) {
  int tid = threadIdx.x; asm volatile("" : "+v"(tid));
  const int lane = tid & 63, w = __builtin_amdgcn_readfirstlane(tid >> 6);
  const unsigned lb = (unsigned)(uintptr_t)lds;
  const unsigned vw0 = (lane >> 3) * 1024 + (((lane & 7) ^ (((lane >> 4) & 1) << 2)) * 16), vw1 = vw0 + 8192, vb = lane * 16;
  const char* wsrc = unip((const char*)wd + ((size_t)fh * 896 + 16 * w) * 1024 + cb * 128);
  const char* hsrc = unip(ws + WS_HF + ((size_t)(4 * tb + w) * 112 + fh * 56) * 1024);
  const int gI = lane >> 4, hh = gI >> 1, q = (lane & 15) >> 2, p = lane & 3;
  const unsigned a0 = lb + (4 * hh + q) * 128 + ((0 ^ ((q >> 1) & 1)) * 64) + (gI & 1) * 32 + p * 8;
  const unsigned a1 = lb + (4 * hh + q) * 128 + ((1 ^ ((q >> 1) & 1)) * 64) + (gI & 1) * 32 + p * 8;
  const unsigned ldsw = uni(lb + w * 2048);
  asm volatile(
      "s_nop 4\n\t"
      "s_mov_b32 s59, m0\n\t"
      "s_add_u32 m0, s50, 0\n\t"
      "s_nop 0\n\t"
      "global_load_lds_dwordx4 v164, s[40:41]\n\t"
      "s_add_u32 m0, s50, 1024\n\t"
      "s_nop 0\n\t"
      "global_load_lds_dwordx4 v165, s[40:41]\n\t"
      "s_add_u32 s40, s40, 0x10000\n\t"
      "s_addc_u32 s41, s41, 0\n\t"
      "s_add_u32 m0, s50, 8192\n\t"
      "s_nop 0\n\t"
      "global_load_lds_dwordx4 v164, s[40:41]\n\t"
      "s_add_u32 m0, s50, 9216\n\t"
      "s_nop 0\n\t"
      "global_load_lds_dwordx4 v165, s[40:41]\n\t"
      "s_add_u32 s40, s40, 0x10000\n\t"
      "s_addc_u32 s41, s41, 0\n\t"
      "s_add_u32 m0, s50, 16384\n\t"
      "s_nop 0\n\t"
      "global_load_lds_dwordx4 v164, s[40:41]\n\t"
      "s_add_u32 m0, s50, 17408\n\t"
      "s_nop 0\n\t"
      "global_load_lds_dwordx4 v165, s[40:41]\n\t"
      "s_add_u32 s40, s40, 0x10000\n\t"
      "s_addc_u32 s41, s41, 0\n\t"
      "s_add_u32 m0, s50, 24576\n\t"
      "s_nop 0\n\t"
      "global_load_lds_dwordx4 v164, s[40:41]\n\t"
      "s_add_u32 m0, s50, 25600\n\t"
      "s_nop 0\n\t"
      "global_load_lds_dwordx4 v165, s[40:41]\n\t"
      "s_add_u32 s40, s40, 0x10000\n\t"
      "s_addc_u32 s41, s41, 0\n\t"
      "s_add_u32 m0, s50, 32768\n\t"
      "s_nop 0\n\t"
      "global_load_lds_dwordx4 v164, s[40:41]\n\t"
      "s_add_u32 m0, s50, 33792\n\t"
      "s_nop 0\n\t"
      "global_load_lds_dwordx4 v165, s[40:41]\n\t"
      "s_add_u32 s40, s40, 0x10000\n\t"
      "s_addc_u32 s41, s41, 0\n\t"
      "s_add_u32 m0, s50, 40960\n\t"
      "s_nop 0\n\t"
      "global_load_lds_dwordx4 v164, s[40:41]\n\t"
      "s_add_u32 m0, s50, 41984\n\t"
      "s_nop 0\n\t"
      "global_load_lds_dwordx4 v165, s[40:41]\n\t"
      "s_add_u32 s40, s40, 0x10000\n\t"
      "s_addc_u32 s41, s41, 0\n\t"
      "s_mov_b32 m0, s59\n\t"
      : "+{s[40:41]}"(wsrc) : "{v164}"(vw0), "{v165}"(vw1), "{s50}"(ldsw) : "s59", "scc", "memory");
  wait_words(ws, WS_HFL + ((tb * 2 + fh) * 4 + w) * 32, 8, tag, lane);
  const char* wosrc = unip((const char*)wo + cb * 128 + (size_t)2 * w * 8192);
  f32x16 c0 = {}, c1 = {};
  asm volatile(
      "s_nop 4\n\t"
      "s_mov_b32 s59, m0\n\t"
      "global_load_dwordx4 v[50:53], v166, s[48:49] offset:0 sc1\n\t"
      "global_load_dwordx4 v[54:57], v166, s[48:49] offset:1024 sc1\n\t"
      "global_load_dwordx4 v[58:61], v166, s[48:49] offset:2048 sc1\n\t"
      "global_load_dwordx4 v[62:65], v166, s[48:49] offset:3072 sc1\n\t"
      "s_add_u32 s48, s48, 4096\n\t"
      "s_addc_u32 s49, s49, 0\n\t"
      "global_load_dwordx4 v[66:69], v166, s[48:49] offset:0 sc1\n\t"
      "global_load_dwordx4 v[70:73], v166, s[48:49] offset:1024 sc1\n\t"
      "global_load_dwordx4 v[74:77], v166, s[48:49] offset:2048 sc1\n\t"
      "global_load_dwordx4 v[78:81], v166, s[48:49] offset:3072 sc1\n\t"
      "s_add_u32 s48, s48, 4096\n\t"
      "s_addc_u32 s49, s49, 0\n\t"
      "global_load_dwordx4 v[82:85], v166, s[48:49] offset:0 sc1\n\t"
      "global_load_dwordx4 v[86:89], v166, s[48:49] offset:1024 sc1\n\t"
      "global_load_dwordx4 v[90:93], v166, s[48:49] offset:2048 sc1\n\t"
      "global_load_dwordx4 v[94:97], v166, s[48:49] offset:3072 sc1\n\t"
      "s_add_u32 s48, s48, 4096\n\t"
      "s_addc_u32 s49, s49, 0\n\t"
      "global_load_dwordx4 v[98:101], v166, s[48:49] offset:0 sc1\n\t"
      "global_load_dwordx4 v[102:105], v166, s[48:49] offset:1024 sc1\n\t"
      "global_load_dwordx4 v[106:109], v166, s[48:49] offset:2048 sc1\n\t"
      "global_load_dwordx4 v[110:113], v166, s[48:49] offset:3072 sc1\n\t"
      "s_add_u32 s48, s48, 4096\n\t"
      "s_addc_u32 s49, s49, 0\n\t"
      "global_load_dwordx4 v[114:117], v166, s[48:49] offset:0 sc1\n\t"
      "global_load_dwordx4 v[118:121], v166, s[48:49] offset:1024 sc1\n\t"
      "global_load_dwordx4 v[122:125], v166, s[48:49] offset:2048 sc1\n\t"
      "global_load_dwordx4 v[126:129], v166, s[48:49] offset:3072 sc1\n\t"
      "s_add_u32 s48, s48, 4096\n\t"
      "s_addc_u32 s49, s49, 0\n\t"
      "global_load_dwordx4 v[130:133], v166, s[48:49] offset:0 sc1\n\t"
      "global_load_dwordx4 v[134:137], v166, s[48:49] offset:1024 sc1\n\t"
      "global_load_dwordx4 v[138:141], v166, s[48:49] offset:2048 sc1\n\t"
      "global_load_dwordx4 v[142:145], v166, s[48:49] offset:3072 sc1\n\t"
      "s_add_u32 s48, s48, 4096\n\t"
      "s_addc_u32 s49, s49, 0\n\t"
      "s_waitcnt vmcnt(20)\n\t"
      "s_barrier\n\t"
      "ds_read_b64_tr_b16 v[34:35], v162 offset:0\n\t"
      "ds_read_b64_tr_b16 v[36:37], v162 offset:1024\n\t"
      "ds_read_b64_tr_b16 v[38:39], v163 offset:0\n\t"
      "ds_read_b64_tr_b16 v[40:41], v163 offset:1024\n\t"
      "ds_read_b64_tr_b16 v[42:43], v162 offset:2048\n\t"
      "ds_read_b64_tr_b16 v[44:45], v162 offset:3072\n\t"
      "s_waitcnt lgkmcnt(4)\n\t"
      "v_mfma_f32_32x32x16_bf16 v[2:17], v[34:37], v[50:53], v[2:17]\n\t"
      "ds_read_b64_tr_b16 v[46:47], v163 offset:2048\n\t"
      "ds_read_b64_tr_b16 v[48:49], v163 offset:3072\n\t"
      "s_waitcnt lgkmcnt(4)\n\t"
      "v_mfma_f32_32x32x16_bf16 v[18:33], v[38:41], v[50:53], v[18:33]\n\t"
      "ds_read_b64_tr_b16 v[34:35], v162 offset:4096\n\t"
      "ds_read_b64_tr_b16 v[36:37], v162 offset:5120\n\t"
      "s_waitcnt lgkmcnt(4)\n\t"
      "v_mfma_f32_32x32x16_bf16 v[2:17], v[42:45], v[54:57], v[2:17]\n\t"
      "ds_read_b64_tr_b16 v[38:39], v163 offset:4096\n\t"
      "ds_read_b64_tr_b16 v[40:41], v163 offset:5120\n\t"
      "s_waitcnt lgkmcnt(4)\n\t"
      "v_mfma_f32_32x32x16_bf16 v[18:33], v[46:49], v[54:57], v[18:33]\n\t"
      "ds_read_b64_tr_b16 v[42:43], v162 offset:6144\n\t"
      "ds_read_b64_tr_b16 v[44:45], v162 offset:7168\n\t"
      "s_waitcnt lgkmcnt(4)\n\t"
      "v_mfma_f32_32x32x16_bf16 v[2:17], v[34:37], v[58:61], v[2:17]\n\t"
      "ds_read_b64_tr_b16 v[46:47], v163 offset:6144\n\t"
      "ds_read_b64_tr_b16 v[48:49], v163 offset:7168\n\t"
      "s_add_u32 m0, s50, 49152\n\t"
      "s_waitcnt lgkmcnt(4)\n\t"
      "v_mfma_f32_32x32x16_bf16 v[18:33], v[38:41], v[58:61], v[18:33]\n\t"
      "s_waitcnt vmcnt(16)\n\t"
      "s_barrier\n\t"
      "ds_read_b64_tr_b16 v[34:35], v162 offset:8192\n\t"
      "ds_read_b64_tr_b16 v[36:37], v162 offset:9216\n\t"
      "global_load_lds_dwordx4 v164, s[40:41]\n\t"
      "s_add_u32 m0, s50, 50176\n\t"
      "s_waitcnt lgkmcnt(4)\n\t"
      "v_mfma_f32_32x32x16_bf16 v[2:17], v[42:45], v[62:65], v[2:17]\n\t"
      "ds_read_b64_tr_b16 v[38:39], v163 offset:8192\n\t"
      "ds_read_b64_tr_b16 v[40:41], v163 offset:9216\n\t"
      "global_load_lds_dwordx4 v165, s[40:41]\n\t"
      "s_add_u32 s40, s40, 0x10000\n\t"
      "s_addc_u32 s41, s41, 0\n\t"
      "s_waitcnt lgkmcnt(4)\n\t"
      "v_mfma_f32_32x32x16_bf16 v[18:33], v[46:49], v[62:65], v[18:33]\n\t"
      "ds_read_b64_tr_b16 v[42:43], v162 offset:10240\n\t"
      "ds_read_b64_tr_b16 v[44:45], v162 offset:11264\n\t"
      "global_load_dwordx4 v[146:149], v166, s[48:49] offset:0 sc1\n\t"
      "s_waitcnt lgkmcnt(4)\n\t"
      "v_mfma_f32_32x32x16_bf16 v[2:17], v[34:37], v[66:69], v[2:17]\n\t"
      "ds_read_b64_tr_b16 v[46:47], v163 offset:10240\n\t"
      "ds_read_b64_tr_b16 v[48:49], v163 offset:11264\n\t"
      "global_load_dwordx4 v[150:153], v166, s[48:49] offset:1024 sc1\n\t"
      "s_waitcnt lgkmcnt(4)\n\t"
      "v_mfma_f32_32x32x16_bf16 v[18:33], v[38:41], v[66:69], v[18:33]\n\t"
      "ds_read_b64_tr_b16 v[34:35], v162 offset:12288\n\t"
      "ds_read_b64_tr_b16 v[36:37], v162 offset:13312\n\t"
      "global_load_dwordx4 v[154:157], v166, s[48:49] offset:2048 sc1\n\t"
      "s_waitcnt lgkmcnt(4)\n\t"
      "v_mfma_f32_32x32x16_bf16 v[2:17], v[42:45], v[70:73], v[2:17]\n\t"
      "ds_read_b64_tr_b16 v[38:39], v163 offset:12288\n\t"
      "ds_read_b64_tr_b16 v[40:41], v163 offset:13312\n\t"
      "global_load_dwordx4 v[158:161], v166, s[48:49] offset:3072 sc1\n\t"
      "s_add_u32 s48, s48, 4096\n\t"
      "s_addc_u32 s49, s49, 0\n\t"
      "s_waitcnt lgkmcnt(4)\n\t"
      "v_mfma_f32_32x32x16_bf16 v[18:33], v[46:49], v[70:73], v[18:33]\n\t"
      "ds_read_b64_tr_b16 v[42:43], v162 offset:14336\n\t"
      "ds_read_b64_tr_b16 v[44:45], v162 offset:15360\n\t"
      "s_waitcnt lgkmcnt(4)\n\t"
      "v_mfma_f32_32x32x16_bf16 v[2:17], v[34:37], v[74:77], v[2:17]\n\t"
      "ds_read_b64_tr_b16 v[46:47], v163 offset:14336\n\t"
      "ds_read_b64_tr_b16 v[48:49], v163 offset:15360\n\t"
      "s_add_u32 m0, s50, 0\n\t"
      "s_waitcnt lgkmcnt(4)\n\t"
      "v_mfma_f32_32x32x16_bf16 v[18:33], v[38:41], v[74:77], v[18:33]\n\t"
      "s_waitcnt vmcnt(18)\n\t"
      "s_barrier\n\t"
      "ds_read_b64_tr_b16 v[34:35], v162 offset:16384\n\t"
      "ds_read_b64_tr_b16 v[36:37], v162 offset:17408\n\t"
      "global_load_lds_dwordx4 v164, s[40:41]\n\t"
      "s_add_u32 m0, s50, 1024\n\t"
      "s_waitcnt lgkmcnt(4)\n\t"
      "v_mfma_f32_32x32x16_bf16 v[2:17], v[42:45], v[78:81], v[2:17]\n\t"
      "ds_read_b64_tr_b16 v[38:39], v163 offset:16384\n\t"
      "ds_read_b64_tr_b16 v[40:41], v163 offset:17408\n\t"
      "global_load_lds_dwordx4 v165, s[40:41]\n\t"
      "s_add_u32 s40, s40, 0x10000\n\t"
      "s_addc_u32 s41, s41, 0\n\t"
      "s_waitcnt lgkmcnt(4)\n\t"
      "v_mfma_f32_32x32x16_bf16 v[18:33], v[46:49], v[78:81], v[18:33]\n\t"
      "ds_read_b64_tr_b16 v[42:43], v162 offset:18432\n\t"
      "ds_read_b64_tr_b16 v[44:45], v162 offset:19456\n\t"
      "global_load_dwordx4 v[50:53], v166, s[48:49] offset:0 sc1\n\t"
      "s_waitcnt lgkmcnt(4)\n\t"
      "v_mfma_f32_32x32x16_bf16 v[2:17], v[34:37], v[82:85], v[2:17]\n\t"
      "ds_read_b64_tr_b16 v[46:47], v163 offset:18432\n\t"
      "ds_read_b64_tr_b16 v[48:49], v163 offset:19456\n\t"
      "global_load_dwordx4 v[54:57], v166, s[48:49] offset:1024 sc1\n\t"
      "s_waitcnt lgkmcnt(4)\n\t"
      "v_mfma_f32_32x32x16_bf16 v[18:33], v[38:41], v[82:85], v[18:33]\n\t"
      "ds_read_b64_tr_b16 v[34:35], v162 offset:20480\n\t"
      "ds_read_b64_tr_b16 v[36:37], v162 offset:21504\n\t"
      "global_load_dwordx4 v[58:61], v166, s[48:49] offset:2048 sc1\n\t"
      "s_waitcnt lgkmcnt(4)\n\t"
      "v_mfma_f32_32x32x16_bf16 v[2:17], v[42:45], v[86:89], v[2:17]\n\t"
      "ds_read_b64_tr_b16 v[38:39], v163 offset:20480\n\t"
      "ds_read_b64_tr_b16 v[40:41], v163 offset:21504\n\t"
      "global_load_dwordx4 v[62:65], v166, s[48:49] offset:3072 sc1\n\t"
      "s_add_u32 s48, s48, 4096\n\t"
      "s_addc_u32 s49, s49, 0\n\t"
      "s_waitcnt lgkmcnt(4)\n\t"
      "v_mfma_f32_32x32x16_bf16 v[18:33], v[46:49], v[86:89], v[18:33]\n\t"
      "ds_read_b64_tr_b16 v[42:43], v162 offset:22528\n\t"
      "ds_read_b64_tr_b16 v[44:45], v162 offset:23552\n\t"
      "s_waitcnt lgkmcnt(4)\n\t"
      "v_mfma_f32_32x32x16_bf16 v[2:17], v[34:37], v[90:93], v[2:17]\n\t"
      "ds_read_b64_tr_b16 v[46:47], v163 offset:22528\n\t"
      "ds_read_b64_tr_b16 v[48:49], v163 offset:23552\n\t"
      "s_add_u32 m0, s50, 8192\n\t"
      "s_waitcnt lgkmcnt(4)\n\t"
      "v_mfma_f32_32x32x16_bf16 v[18:33], v[38:41], v[90:93], v[18:33]\n\t"
      "s_waitcnt vmcnt(20)\n\t"
      "s_barrier\n\t"
      "ds_read_b64_tr_b16 v[34:35], v162 offset:24576\n\t"
      "ds_read_b64_tr_b16 v[36:37], v162 offset:25600\n\t"
      "global_load_lds_dwordx4 v164, s[40:41]\n\t"
      "s_add_u32 m0, s50, 9216\n\t"
      "s_waitcnt lgkmcnt(4)\n\t"
      "v_mfma_f32_32x32x16_bf16 v[2:17], v[42:45], v[94:97], v[2:17]\n\t"
      "ds_read_b64_tr_b16 v[38:39], v163 offset:24576\n\t"
      "ds_read_b64_tr_b16 v[40:41], v163 offset:25600\n\t"
      "global_load_lds_dwordx4 v165, s[40:41]\n\t"
      "s_add_u32 s40, s40, 0x10000\n\t"
      "s_addc_u32 s41, s41, 0\n\t"
      "s_waitcnt lgkmcnt(4)\n\t"
      "v_mfma_f32_32x32x16_bf16 v[18:33], v[46:49], v[94:97], v[18:33]\n\t"
      "ds_read_b64_tr_b16 v[42:43], v162 offset:26624\n\t"
      "ds_read_b64_tr_b16 v[44:45], v162 offset:27648\n\t"
      "global_load_dwordx4 v[66:69], v166, s[48:49] offset:0 sc1\n\t"
      "s_waitcnt lgkmcnt(4)\n\t"
      "v_mfma_f32_32x32x16_bf16 v[2:17], v[34:37], v[98:101], v[2:17]\n\t"
      "ds_read_b64_tr_b16 v[46:47], v163 offset:26624\n\t"
      "ds_read_b64_tr_b16 v[48:49], v163 offset:27648\n\t"
      "global_load_dwordx4 v[70:73], v166, s[48:49] offset:1024 sc1\n\t"
      "s_waitcnt lgkmcnt(4)\n\t"
      "v_mfma_f32_32x32x16_bf16 v[18:33], v[38:41], v[98:101], v[18:33]\n\t"
      "ds_read_b64_tr_b16 v[34:35], v162 offset:28672\n\t"
      "ds_read_b64_tr_b16 v[36:37], v162 offset:29696\n\t"
      "global_load_dwordx4 v[74:77], v166, s[48:49] offset:2048 sc1\n\t"
      "s_waitcnt lgkmcnt(4)\n\t"
      "v_mfma_f32_32x32x16_bf16 v[2:17], v[42:45], v[102:105], v[2:17]\n\t"
      "ds_read_b64_tr_b16 v[38:39], v163 offset:28672\n\t"
      "ds_read_b64_tr_b16 v[40:41], v163 offset:29696\n\t"
      "global_load_dwordx4 v[78:81], v166, s[48:49] offset:3072 sc1\n\t"
      "s_add_u32 s48, s48, 4096\n\t"
      "s_addc_u32 s49, s49, 0\n\t"
      "s_waitcnt lgkmcnt(4)\n\t"
      "v_mfma_f32_32x32x16_bf16 v[18:33], v[46:49], v[102:105], v[18:33]\n\t"
      "ds_read_b64_tr_b16 v[42:43], v162 offset:30720\n\t"
      "ds_read_b64_tr_b16 v[44:45], v162 offset:31744\n\t"
      "s_waitcnt lgkmcnt(4)\n\t"
      "v_mfma_f32_32x32x16_bf16 v[2:17], v[34:37], v[106:109], v[2:17]\n\t"
      "ds_read_b64_tr_b16 v[46:47], v163 offset:30720\n\t"
      "ds_read_b64_tr_b16 v[48:49], v163 offset:31744\n\t"
      "s_add_u32 m0, s50, 16384\n\t"
      "s_waitcnt lgkmcnt(4)\n\t"
      "v_mfma_f32_32x32x16_bf16 v[18:33], v[38:41], v[106:109], v[18:33]\n\t"
      "s_waitcnt vmcnt(22)\n\t"
      "s_barrier\n\t"
      "ds_read_b64_tr_b16 v[34:35], v162 offset:32768\n\t"
      "ds_read_b64_tr_b16 v[36:37], v162 offset:33792\n\t"
      "global_load_lds_dwordx4 v164, s[40:41]\n\t"
      "s_add_u32 m0, s50, 17408\n\t"
      "s_waitcnt lgkmcnt(4)\n\t"
      "v_mfma_f32_32x32x16_bf16 v[2:17], v[42:45], v[110:113], v[2:17]\n\t"
      "ds_read_b64_tr_b16 v[38:39], v163 offset:32768\n\t"
      "ds_read_b64_tr_b16 v[40:41], v163 offset:33792\n\t"
      "global_load_lds_dwordx4 v165, s[40:41]\n\t"
      "s_add_u32 s40, s40, 0x10000\n\t"
      "s_addc_u32 s41, s41, 0\n\t"
      "s_waitcnt lgkmcnt(4)\n\t"
      "v_mfma_f32_32x32x16_bf16 v[18:33], v[46:49], v[110:113], v[18:33]\n\t"
      "ds_read_b64_tr_b16 v[42:43], v162 offset:34816\n\t"
      "ds_read_b64_tr_b16 v[44:45], v162 offset:35840\n\t"
      "global_load_dwordx4 v[82:85], v166, s[48:49] offset:0 sc1\n\t"
      "s_waitcnt lgkmcnt(4)\n\t"
      "v_mfma_f32_32x32x16_bf16 v[2:17], v[34:37], v[114:117], v[2:17]\n\t"
      "ds_read_b64_tr_b16 v[46:47], v163 offset:34816\n\t"
      "ds_read_b64_tr_b16 v[48:49], v163 offset:35840\n\t"
      "global_load_dwordx4 v[86:89], v166, s[48:49] offset:1024 sc1\n\t"
      "s_waitcnt lgkmcnt(4)\n\t"
      "v_mfma_f32_32x32x16_bf16 v[18:33], v[38:41], v[114:117], v[18:33]\n\t"
      "ds_read_b64_tr_b16 v[34:35], v162 offset:36864\n\t"
      "ds_read_b64_tr_b16 v[36:37], v162 offset:37888\n\t"
      "global_load_dwordx4 v[90:93], v166, s[48:49] offset:2048 sc1\n\t"
      "s_waitcnt lgkmcnt(4)\n\t"
      "v_mfma_f32_32x32x16_bf16 v[2:17], v[42:45], v[118:121], v[2:17]\n\t"
      "ds_read_b64_tr_b16 v[38:39], v163 offset:36864\n\t"
      "ds_read_b64_tr_b16 v[40:41], v163 offset:37888\n\t"
      "global_load_dwordx4 v[94:97], v166, s[48:49] offset:3072 sc1\n\t"
      "s_add_u32 s48, s48, 4096\n\t"
      "s_addc_u32 s49, s49, 0\n\t"
      "s_waitcnt lgkmcnt(4)\n\t"
      "v_mfma_f32_32x32x16_bf16 v[18:33], v[46:49], v[118:121], v[18:33]\n\t"
      "ds_read_b64_tr_b16 v[42:43], v162 offset:38912\n\t"
      "ds_read_b64_tr_b16 v[44:45], v162 offset:39936\n\t"
      "s_waitcnt lgkmcnt(4)\n\t"
      "v_mfma_f32_32x32x16_bf16 v[2:17], v[34:37], v[122:125], v[2:17]\n\t"
      "ds_read_b64_tr_b16 v[46:47], v163 offset:38912\n\t"
      "ds_read_b64_tr_b16 v[48:49], v163 offset:39936\n\t"
      "s_add_u32 m0, s50, 24576\n\t"
      "s_waitcnt lgkmcnt(4)\n\t"
      "v_mfma_f32_32x32x16_bf16 v[18:33], v[38:41], v[122:125], v[18:33]\n\t"
      "s_waitcnt vmcnt(24)\n\t"
      "s_barrier\n\t"
      "ds_read_b64_tr_b16 v[34:35], v162 offset:40960\n\t"
      "ds_read_b64_tr_b16 v[36:37], v162 offset:41984\n\t"
      "global_load_lds_dwordx4 v164, s[40:41]\n\t"
      "s_add_u32 m0, s50, 25600\n\t"
      "s_waitcnt lgkmcnt(4)\n\t"
      "v_mfma_f32_32x32x16_bf16 v[2:17], v[42:45], v[126:129], v[2:17]\n\t"
      "ds_read_b64_tr_b16 v[38:39], v163 offset:40960\n\t"
      "ds_read_b64_tr_b16 v[40:41], v163 offset:41984\n\t"
      "global_load_lds_dwordx4 v165, s[40:41]\n\t"
      "s_add_u32 s40, s40, 0x10000\n\t"
      "s_addc_u32 s41, s41, 0\n\t"
      "s_waitcnt lgkmcnt(4)\n\t"
      "v_mfma_f32_32x32x16_bf16 v[18:33], v[46:49], v[126:129], v[18:33]\n\t"
      "ds_read_b64_tr_b16 v[42:43], v162 offset:43008\n\t"
      "ds_read_b64_tr_b16 v[44:45], v162 offset:44032\n\t"
      "global_load_dwordx4 v[98:101], v166, s[48:49] offset:0 sc1\n\t"
      "s_waitcnt lgkmcnt(4)\n\t"
      "v_mfma_f32_32x32x16_bf16 v[2:17], v[34:37], v[130:133], v[2:17]\n\t"
      "ds_read_b64_tr_b16 v[46:47], v163 offset:43008\n\t"
      "ds_read_b64_tr_b16 v[48:49], v163 offset:44032\n\t"
      "global_load_dwordx4 v[102:105], v166, s[48:49] offset:1024 sc1\n\t"
      "s_waitcnt lgkmcnt(4)\n\t"
      "v_mfma_f32_32x32x16_bf16 v[18:33], v[38:41], v[130:133], v[18:33]\n\t"
      "ds_read_b64_tr_b16 v[34:35], v162 offset:45056\n\t"
      "ds_read_b64_tr_b16 v[36:37], v162 offset:46080\n\t"
      "global_load_dwordx4 v[106:109], v166, s[48:49] offset:2048 sc1\n\t"
      "s_waitcnt lgkmcnt(4)\n\t"
      "v_mfma_f32_32x32x16_bf16 v[2:17], v[42:45], v[134:137], v[2:17]\n\t"
      "ds_read_b64_tr_b16 v[38:39], v163 offset:45056\n\t"
      "ds_read_b64_tr_b16 v[40:41], v163 offset:46080\n\t"
      "global_load_dwordx4 v[110:113], v166, s[48:49] offset:3072 sc1\n\t"
      "s_add_u32 s48, s48, 4096\n\t"
      "s_addc_u32 s49, s49, 0\n\t"
      "s_waitcnt lgkmcnt(4)\n\t"
      "v_mfma_f32_32x32x16_bf16 v[18:33], v[46:49], v[134:137], v[18:33]\n\t"
      "ds_read_b64_tr_b16 v[42:43], v162 offset:47104\n\t"
      "ds_read_b64_tr_b16 v[44:45], v162 offset:48128\n\t"
      "s_waitcnt lgkmcnt(4)\n\t"
      "v_mfma_f32_32x32x16_bf16 v[2:17], v[34:37], v[138:141], v[2:17]\n\t"
      "ds_read_b64_tr_b16 v[46:47], v163 offset:47104\n\t"
      "ds_read_b64_tr_b16 v[48:49], v163 offset:48128\n\t"
      "s_add_u32 m0, s50, 32768\n\t"
      "s_waitcnt lgkmcnt(4)\n\t"
      "v_mfma_f32_32x32x16_bf16 v[18:33], v[38:41], v[138:141], v[18:33]\n\t"
      "s_waitcnt vmcnt(24)\n\t"
      "s_barrier\n\t"
      "ds_read_b64_tr_b16 v[34:35], v162 offset:49152\n\t"
      "ds_read_b64_tr_b16 v[36:37], v162 offset:50176\n\t"
      "global_load_lds_dwordx4 v164, s[40:41]\n\t"
      "s_add_u32 m0, s50, 33792\n\t"
      "s_waitcnt lgkmcnt(4)\n\t"
      "v_mfma_f32_32x32x16_bf16 v[2:17], v[42:45], v[142:145], v[2:17]\n\t"
      "ds_read_b64_tr_b16 v[38:39], v163 offset:49152\n\t"
      "ds_read_b64_tr_b16 v[40:41], v163 offset:50176\n\t"
      "global_load_lds_dwordx4 v165, s[40:41]\n\t"
      "s_add_u32 s40, s40, 0x10000\n\t"
      "s_addc_u32 s41, s41, 0\n\t"
      "s_waitcnt lgkmcnt(4)\n\t"
      "v_mfma_f32_32x32x16_bf16 v[18:33], v[46:49], v[142:145], v[18:33]\n\t"
      "ds_read_b64_tr_b16 v[42:43], v162 offset:51200\n\t"
      "ds_read_b64_tr_b16 v[44:45], v162 offset:52224\n\t"
      "global_load_dwordx4 v[114:117], v166, s[48:49] offset:0 sc1\n\t"
      "s_waitcnt lgkmcnt(4)\n\t"
      "v_mfma_f32_32x32x16_bf16 v[2:17], v[34:37], v[146:149], v[2:17]\n\t"
      "ds_read_b64_tr_b16 v[46:47], v163 offset:51200\n\t"
      "ds_read_b64_tr_b16 v[48:49], v163 offset:52224\n\t"
      "global_load_dwordx4 v[118:121], v166, s[48:49] offset:1024 sc1\n\t"
      "s_waitcnt lgkmcnt(4)\n\t"
      "v_mfma_f32_32x32x16_bf16 v[18:33], v[38:41], v[146:149], v[18:33]\n\t"
      "ds_read_b64_tr_b16 v[34:35], v162 offset:53248\n\t"
      "ds_read_b64_tr_b16 v[36:37], v162 offset:54272\n\t"
      "global_load_dwordx4 v[122:125], v166, s[48:49] offset:2048 sc1\n\t"
      "s_waitcnt lgkmcnt(4)\n\t"
      "v_mfma_f32_32x32x16_bf16 v[2:17], v[42:45], v[150:153], v[2:17]\n\t"
      "ds_read_b64_tr_b16 v[38:39], v163 offset:53248\n\t"
      "ds_read_b64_tr_b16 v[40:41], v163 offset:54272\n\t"
      "global_load_dwordx4 v[126:129], v166, s[48:49] offset:3072 sc1\n\t"
      "s_add_u32 s48, s48, 4096\n\t"
      "s_addc_u32 s49, s49, 0\n\t"
      "s_waitcnt lgkmcnt(4)\n\t"
      "v_mfma_f32_32x32x16_bf16 v[18:33], v[46:49], v[150:153], v[18:33]\n\t"
      "ds_read_b64_tr_b16 v[42:43], v162 offset:55296\n\t"
      "ds_read_b64_tr_b16 v[44:45], v162 offset:56320\n\t"
      "s_waitcnt lgkmcnt(4)\n\t"
      "v_mfma_f32_32x32x16_bf16 v[2:17], v[34:37], v[154:157], v[2:17]\n\t"
      "ds_read_b64_tr_b16 v[46:47], v163 offset:55296\n\t"
      "ds_read_b64_tr_b16 v[48:49], v163 offset:56320\n\t"
      "s_add_u32 m0, s50, 40960\n\t"
      "s_waitcnt lgkmcnt(4)\n\t"
      "v_mfma_f32_32x32x16_bf16 v[18:33], v[38:41], v[154:157], v[18:33]\n\t"
      "s_waitcnt vmcnt(24)\n\t"
      "s_barrier\n\t"
      "ds_read_b64_tr_b16 v[34:35], v162 offset:0\n\t"
      "ds_read_b64_tr_b16 v[36:37], v162 offset:1024\n\t"
      "global_load_lds_dwordx4 v164, s[40:41]\n\t"
      "s_add_u32 m0, s50, 41984\n\t"
      "s_waitcnt lgkmcnt(4)\n\t"
      "v_mfma_f32_32x32x16_bf16 v[2:17], v[42:45], v[158:161], v[2:17]\n\t"
      "ds_read_b64_tr_b16 v[38:39], v163 offset:0\n\t"
      "ds_read_b64_tr_b16 v[40:41], v163 offset:1024\n\t"
      "global_load_lds_dwordx4 v165, s[40:41]\n\t"
      "s_add_u32 s40, s40, 0x10000\n\t"
      "s_addc_u32 s41, s41, 0\n\t"
      "s_waitcnt lgkmcnt(4)\n\t"
      "v_mfma_f32_32x32x16_bf16 v[18:33], v[46:49], v[158:161], v[18:33]\n\t"
      "ds_read_b64_tr_b16 v[42:43], v162 offset:2048\n\t"
      "ds_read_b64_tr_b16 v[44:45], v162 offset:3072\n\t"
      "global_load_dwordx4 v[130:133], v166, s[48:49] offset:0 sc1\n\t"
      "s_waitcnt lgkmcnt(4)\n\t"
      "v_mfma_f32_32x32x16_bf16 v[2:17], v[34:37], v[50:53], v[2:17]\n\t"
      "ds_read_b64_tr_b16 v[46:47], v163 offset:2048\n\t"
      "ds_read_b64_tr_b16 v[48:49], v163 offset:3072\n\t"
      "global_load_dwordx4 v[134:137], v166, s[48:49] offset:1024 sc1\n\t"
      "s_waitcnt lgkmcnt(4)\n\t"
      "v_mfma_f32_32x32x16_bf16 v[18:33], v[38:41], v[50:53], v[18:33]\n\t"
      "ds_read_b64_tr_b16 v[34:35], v162 offset:4096\n\t"
      "ds_read_b64_tr_b16 v[36:37], v162 offset:5120\n\t"
      "global_load_dwordx4 v[138:141], v166, s[48:49] offset:2048 sc1\n\t"
      "s_waitcnt lgkmcnt(4)\n\t"
      "v_mfma_f32_32x32x16_bf16 v[2:17], v[42:45], v[54:57], v[2:17]\n\t"
      "ds_read_b64_tr_b16 v[38:39], v163 offset:4096\n\t"
      "ds_read_b64_tr_b16 v[40:41], v163 offset:5120\n\t"
      "global_load_dwordx4 v[142:145], v166, s[48:49] offset:3072 sc1\n\t"
      "s_add_u32 s48, s48, 4096\n\t"
      "s_addc_u32 s49, s49, 0\n\t"
      "s_waitcnt lgkmcnt(4)\n\t"
      "v_mfma_f32_32x32x16_bf16 v[18:33], v[46:49], v[54:57], v[18:33]\n\t"
      "ds_read_b64_tr_b16 v[42:43], v162 offset:6144\n\t"
      "ds_read_b64_tr_b16 v[44:45], v162 offset:7168\n\t"
      "s_waitcnt lgkmcnt(4)\n\t"
      "v_mfma_f32_32x32x16_bf16 v[2:17], v[34:37], v[58:61], v[2:17]\n\t"
      "ds_read_b64_tr_b16 v[46:47], v163 offset:6144\n\t"
      "ds_read_b64_tr_b16 v[48:49], v163 offset:7168\n\t"
      "s_add_u32 m0, s50, 49152\n\t"
      "s_waitcnt lgkmcnt(4)\n\t"
      "v_mfma_f32_32x32x16_bf16 v[18:33], v[38:41], v[58:61], v[18:33]\n\t"
      "s_waitcnt vmcnt(24)\n\t"
      "s_barrier\n\t"
      "ds_read_b64_tr_b16 v[34:35], v162 offset:8192\n\t"
      "ds_read_b64_tr_b16 v[36:37], v162 offset:9216\n\t"
      "global_load_lds_dwordx4 v164, s[40:41]\n\t"
      "s_add_u32 m0, s50, 50176\n\t"
      "s_waitcnt lgkmcnt(4)\n\t"
      "v_mfma_f32_32x32x16_bf16 v[2:17], v[42:45], v[62:65], v[2:17]\n\t"
      "ds_read_b64_tr_b16 v[38:39], v163 offset:8192\n\t"
      "ds_read_b64_tr_b16 v[40:41], v163 offset:9216\n\t"
      "global_load_lds_dwordx4 v165, s[40:41]\n\t"
      "s_add_u32 s40, s40, 0x10000\n\t"
      "s_addc_u32 s41, s41, 0\n\t"
      "s_waitcnt lgkmcnt(4)\n\t"
      "v_mfma_f32_32x32x16_bf16 v[18:33], v[46:49], v[62:65], v[18:33]\n\t"
      "ds_read_b64_tr_b16 v[42:43], v162 offset:10240\n\t"
      "ds_read_b64_tr_b16 v[44:45], v162 offset:11264\n\t"
      "global_load_dwordx4 v[146:149], v166, s[48:49] offset:0 sc1\n\t"
      "s_waitcnt lgkmcnt(4)\n\t"
      "v_mfma_f32_32x32x16_bf16 v[2:17], v[34:37], v[66:69], v[2:17]\n\t"
      "ds_read_b64_tr_b16 v[46:47], v163 offset:10240\n\t"
      "ds_read_b64_tr_b16 v[48:49], v163 offset:11264\n\t"
      "global_load_dwordx4 v[150:153], v166, s[48:49] offset:1024 sc1\n\t"
      "s_waitcnt lgkmcnt(4)\n\t"
      "v_mfma_f32_32x32x16_bf16 v[18:33], v[38:41], v[66:69], v[18:33]\n\t"
      "ds_read_b64_tr_b16 v[34:35], v162 offset:12288\n\t"
      "ds_read_b64_tr_b16 v[36:37], v162 offset:13312\n\t"
      "global_load_dwordx4 v[154:157], v166, s[48:49] offset:2048 sc1\n\t"
      "s_waitcnt lgkmcnt(4)\n\t"
      "v_mfma_f32_32x32x16_bf16 v[2:17], v[42:45], v[70:73], v[2:17]\n\t"
      "ds_read_b64_tr_b16 v[38:39], v163 offset:12288\n\t"
      "ds_read_b64_tr_b16 v[40:41], v163 offset:13312\n\t"
      "global_load_dwordx4 v[158:161], v166, s[48:49] offset:3072 sc1\n\t"
      "s_add_u32 s48, s48, 4096\n\t"
      "s_addc_u32 s49, s49, 0\n\t"
      "s_waitcnt lgkmcnt(4)\n\t"
      "v_mfma_f32_32x32x16_bf16 v[18:33], v[46:49], v[70:73], v[18:33]\n\t"
      "ds_read_b64_tr_b16 v[42:43], v162 offset:14336\n\t"
      "ds_read_b64_tr_b16 v[44:45], v162 offset:15360\n\t"
      "s_waitcnt lgkmcnt(4)\n\t"
      "v_mfma_f32_32x32x16_bf16 v[2:17], v[34:37], v[74:77], v[2:17]\n\t"
      "ds_read_b64_tr_b16 v[46:47], v163 offset:14336\n\t"
      "ds_read_b64_tr_b16 v[48:49], v163 offset:15360\n\t"
      "s_add_u32 s46, s44, 0\n\t"
      "s_addc_u32 s47, s45, 0\n\t"
      "s_add_u32 m0, s50, 0\n\t"
      "s_waitcnt lgkmcnt(4)\n\t"
      "v_mfma_f32_32x32x16_bf16 v[18:33], v[38:41], v[74:77], v[18:33]\n\t"
      "s_waitcnt vmcnt(24)\n\t"
      "s_barrier\n\t"
      "ds_read_b64_tr_b16 v[34:35], v162 offset:16384\n\t"
      "ds_read_b64_tr_b16 v[36:37], v162 offset:17408\n\t"
      "global_load_lds_dwordx4 v164, s[46:47]\n\t"
      "s_add_u32 m0, s50, 1024\n\t"
      "s_waitcnt lgkmcnt(4)\n\t"
      "v_mfma_f32_32x32x16_bf16 v[2:17], v[42:45], v[78:81], v[2:17]\n\t"
      "ds_read_b64_tr_b16 v[38:39], v163 offset:16384\n\t"
      "ds_read_b64_tr_b16 v[40:41], v163 offset:17408\n\t"
      "global_load_lds_dwordx4 v165, s[46:47]\n\t"
      "s_add_u32 s46, s44, 458752\n\t"
      "s_addc_u32 s47, s45, 0\n\t"
      "s_add_u32 m0, s50, 57344\n\t"
      "s_waitcnt lgkmcnt(4)\n\t"
      "v_mfma_f32_32x32x16_bf16 v[18:33], v[46:49], v[78:81], v[18:33]\n\t"
      "ds_read_b64_tr_b16 v[42:43], v162 offset:18432\n\t"
      "ds_read_b64_tr_b16 v[44:45], v162 offset:19456\n\t"
      "global_load_lds_dwordx4 v164, s[46:47]\n\t"
      "s_add_u32 m0, s50, 58368\n\t"
      "s_waitcnt lgkmcnt(4)\n\t"
      "v_mfma_f32_32x32x16_bf16 v[2:17], v[34:37], v[82:85], v[2:17]\n\t"
      "ds_read_b64_tr_b16 v[46:47], v163 offset:18432\n\t"
      "ds_read_b64_tr_b16 v[48:49], v163 offset:19456\n\t"
      "global_load_lds_dwordx4 v165, s[46:47]\n\t"
      "s_waitcnt lgkmcnt(4)\n\t"
      "v_mfma_f32_32x32x16_bf16 v[18:33], v[38:41], v[82:85], v[18:33]\n\t"
      "ds_read_b64_tr_b16 v[34:35], v162 offset:20480\n\t"
      "ds_read_b64_tr_b16 v[36:37], v162 offset:21504\n\t"
      "s_waitcnt lgkmcnt(4)\n\t"
      "v_mfma_f32_32x32x16_bf16 v[2:17], v[42:45], v[86:89], v[2:17]\n\t"
      "ds_read_b64_tr_b16 v[38:39], v163 offset:20480\n\t"
      "ds_read_b64_tr_b16 v[40:41], v163 offset:21504\n\t"
      "s_waitcnt lgkmcnt(4)\n\t"
      "v_mfma_f32_32x32x16_bf16 v[18:33], v[46:49], v[86:89], v[18:33]\n\t"
      "ds_read_b64_tr_b16 v[42:43], v162 offset:22528\n\t"
      "ds_read_b64_tr_b16 v[44:45], v162 offset:23552\n\t"
      "s_waitcnt lgkmcnt(4)\n\t"
      "v_mfma_f32_32x32x16_bf16 v[2:17], v[34:37], v[90:93], v[2:17]\n\t"
      "ds_read_b64_tr_b16 v[46:47], v163 offset:22528\n\t"
      "ds_read_b64_tr_b16 v[48:49], v163 offset:23552\n\t"
      "s_add_u32 s46, s44, 65536\n\t"
      "s_addc_u32 s47, s45, 0\n\t"
      "s_add_u32 m0, s50, 8192\n\t"
      "s_waitcnt lgkmcnt(4)\n\t"
      "v_mfma_f32_32x32x16_bf16 v[18:33], v[38:41], v[90:93], v[18:33]\n\t"
      "s_waitcnt vmcnt(22)\n\t"
      "s_barrier\n\t"
      "ds_read_b64_tr_b16 v[34:35], v162 offset:24576\n\t"
      "ds_read_b64_tr_b16 v[36:37], v162 offset:25600\n\t"
      "global_load_lds_dwordx4 v164, s[46:47]\n\t"
      "s_add_u32 m0, s50, 9216\n\t"
      "s_waitcnt lgkmcnt(4)\n\t"
      "v_mfma_f32_32x32x16_bf16 v[2:17], v[42:45], v[94:97], v[2:17]\n\t"
      "ds_read_b64_tr_b16 v[38:39], v163 offset:24576\n\t"
      "ds_read_b64_tr_b16 v[40:41], v163 offset:25600\n\t"
      "global_load_lds_dwordx4 v165, s[46:47]\n\t"
      "s_waitcnt lgkmcnt(4)\n\t"
      "v_mfma_f32_32x32x16_bf16 v[18:33], v[46:49], v[94:97], v[18:33]\n\t"
      "ds_read_b64_tr_b16 v[42:43], v162 offset:26624\n\t"
      "ds_read_b64_tr_b16 v[44:45], v162 offset:27648\n\t"
      "s_waitcnt lgkmcnt(4)\n\t"
      "v_mfma_f32_32x32x16_bf16 v[2:17], v[34:37], v[98:101], v[2:17]\n\t"
      "ds_read_b64_tr_b16 v[46:47], v163 offset:26624\n\t"
      "ds_read_b64_tr_b16 v[48:49], v163 offset:27648\n\t"
      "s_waitcnt lgkmcnt(4)\n\t"
      "v_mfma_f32_32x32x16_bf16 v[18:33], v[38:41], v[98:101], v[18:33]\n\t"
      "ds_read_b64_tr_b16 v[34:35], v162 offset:28672\n\t"
      "ds_read_b64_tr_b16 v[36:37], v162 offset:29696\n\t"
      "s_waitcnt lgkmcnt(4)\n\t"
      "v_mfma_f32_32x32x16_bf16 v[2:17], v[42:45], v[102:105], v[2:17]\n\t"
      "ds_read_b64_tr_b16 v[38:39], v163 offset:28672\n\t"
      "ds_read_b64_tr_b16 v[40:41], v163 offset:29696\n\t"
      "s_waitcnt lgkmcnt(4)\n\t"
      "v_mfma_f32_32x32x16_bf16 v[18:33], v[46:49], v[102:105], v[18:33]\n\t"
      "ds_read_b64_tr_b16 v[42:43], v162 offset:30720\n\t"
      "ds_read_b64_tr_b16 v[44:45], v162 offset:31744\n\t"
      "s_waitcnt lgkmcnt(4)\n\t"
      "v_mfma_f32_32x32x16_bf16 v[2:17], v[34:37], v[106:109], v[2:17]\n\t"
      "ds_read_b64_tr_b16 v[46:47], v163 offset:30720\n\t"
      "ds_read_b64_tr_b16 v[48:49], v163 offset:31744\n\t"
      "s_add_u32 s46, s44, 131072\n\t"
      "s_addc_u32 s47, s45, 0\n\t"
      "s_add_u32 m0, s50, 16384\n\t"
      "s_waitcnt lgkmcnt(4)\n\t"
      "v_mfma_f32_32x32x16_bf16 v[18:33], v[38:41], v[106:109], v[18:33]\n\t"
      "s_waitcnt vmcnt(18)\n\t"
      "s_barrier\n\t"
      "ds_read_b64_tr_b16 v[34:35], v162 offset:32768\n\t"
      "ds_read_b64_tr_b16 v[36:37], v162 offset:33792\n\t"
      "global_load_lds_dwordx4 v164, s[46:47]\n\t"
      "s_add_u32 m0, s50, 17408\n\t"
      "s_waitcnt lgkmcnt(4)\n\t"
      "v_mfma_f32_32x32x16_bf16 v[2:17], v[42:45], v[110:113], v[2:17]\n\t"
      "ds_read_b64_tr_b16 v[38:39], v163 offset:32768\n\t"
      "ds_read_b64_tr_b16 v[40:41], v163 offset:33792\n\t"
      "global_load_lds_dwordx4 v165, s[46:47]\n\t"
      "s_waitcnt lgkmcnt(4)\n\t"
      "v_mfma_f32_32x32x16_bf16 v[18:33], v[46:49], v[110:113], v[18:33]\n\t"
      "ds_read_b64_tr_b16 v[42:43], v162 offset:34816\n\t"
      "ds_read_b64_tr_b16 v[44:45], v162 offset:35840\n\t"
      "s_waitcnt lgkmcnt(4)\n\t"
      "v_mfma_f32_32x32x16_bf16 v[2:17], v[34:37], v[114:117], v[2:17]\n\t"
      "ds_read_b64_tr_b16 v[46:47], v163 offset:34816\n\t"
      "ds_read_b64_tr_b16 v[48:49], v163 offset:35840\n\t"
      "s_waitcnt lgkmcnt(4)\n\t"
      "v_mfma_f32_32x32x16_bf16 v[18:33], v[38:41], v[114:117], v[18:33]\n\t"
      "ds_read_b64_tr_b16 v[34:35], v162 offset:36864\n\t"
      "ds_read_b64_tr_b16 v[36:37], v162 offset:37888\n\t"
      "s_waitcnt lgkmcnt(4)\n\t"
      "v_mfma_f32_32x32x16_bf16 v[2:17], v[42:45], v[118:121], v[2:17]\n\t"
      "ds_read_b64_tr_b16 v[38:39], v163 offset:36864\n\t"
      "ds_read_b64_tr_b16 v[40:41], v163 offset:37888\n\t"
      "s_waitcnt lgkmcnt(4)\n\t"
      "v_mfma_f32_32x32x16_bf16 v[18:33], v[46:49], v[118:121], v[18:33]\n\t"
      "ds_read_b64_tr_b16 v[42:43], v162 offset:38912\n\t"
      "ds_read_b64_tr_b16 v[44:45], v162 offset:39936\n\t"
      "s_waitcnt lgkmcnt(4)\n\t"
      "v_mfma_f32_32x32x16_bf16 v[2:17], v[34:37], v[122:125], v[2:17]\n\t"
      "ds_read_b64_tr_b16 v[46:47], v163 offset:38912\n\t"
      "ds_read_b64_tr_b16 v[48:49], v163 offset:39936\n\t"
      "s_add_u32 s46, s44, 196608\n\t"
      "s_addc_u32 s47, s45, 0\n\t"
      "s_add_u32 m0, s50, 24576\n\t"
      "s_waitcnt lgkmcnt(4)\n\t"
      "v_mfma_f32_32x32x16_bf16 v[18:33], v[38:41], v[122:125], v[18:33]\n\t"
      "s_waitcnt vmcnt(14)\n\t"
      "s_barrier\n\t"
      "ds_read_b64_tr_b16 v[34:35], v162 offset:40960\n\t"
      "ds_read_b64_tr_b16 v[36:37], v162 offset:41984\n\t"
      "global_load_lds_dwordx4 v164, s[46:47]\n\t"
      "s_add_u32 m0, s50, 25600\n\t"
      "s_waitcnt lgkmcnt(4)\n\t"
      "v_mfma_f32_32x32x16_bf16 v[2:17], v[42:45], v[126:129], v[2:17]\n\t"
      "ds_read_b64_tr_b16 v[38:39], v163 offset:40960\n\t"
      "ds_read_b64_tr_b16 v[40:41], v163 offset:41984\n\t"
      "global_load_lds_dwordx4 v165, s[46:47]\n\t"
      "s_waitcnt lgkmcnt(4)\n\t"
      "v_mfma_f32_32x32x16_bf16 v[18:33], v[46:49], v[126:129], v[18:33]\n\t"
      "ds_read_b64_tr_b16 v[42:43], v162 offset:43008\n\t"
      "ds_read_b64_tr_b16 v[44:45], v162 offset:44032\n\t"
      "s_waitcnt lgkmcnt(4)\n\t"
      "v_mfma_f32_32x32x16_bf16 v[2:17], v[34:37], v[130:133], v[2:17]\n\t"
      "ds_read_b64_tr_b16 v[46:47], v163 offset:43008\n\t"
      "ds_read_b64_tr_b16 v[48:49], v163 offset:44032\n\t"
      "s_waitcnt lgkmcnt(4)\n\t"
      "v_mfma_f32_32x32x16_bf16 v[18:33], v[38:41], v[130:133], v[18:33]\n\t"
      "ds_read_b64_tr_b16 v[34:35], v162 offset:45056\n\t"
      "ds_read_b64_tr_b16 v[36:37], v162 offset:46080\n\t"
      "s_waitcnt lgkmcnt(4)\n\t"
      "v_mfma_f32_32x32x16_bf16 v[2:17], v[42:45], v[134:137], v[2:17]\n\t"
      "ds_read_b64_tr_b16 v[38:39], v163 offset:45056\n\t"
      "ds_read_b64_tr_b16 v[40:41], v163 offset:46080\n\t"
      "s_waitcnt lgkmcnt(4)\n\t"
      "v_mfma_f32_32x32x16_bf16 v[18:33], v[46:49], v[134:137], v[18:33]\n\t"
      "ds_read_b64_tr_b16 v[42:43], v162 offset:47104\n\t"
      "ds_read_b64_tr_b16 v[44:45], v162 offset:48128\n\t"
      "s_waitcnt lgkmcnt(4)\n\t"
      "v_mfma_f32_32x32x16_bf16 v[2:17], v[34:37], v[138:141], v[2:17]\n\t"
      "ds_read_b64_tr_b16 v[46:47], v163 offset:47104\n\t"
      "ds_read_b64_tr_b16 v[48:49], v163 offset:48128\n\t"
      "s_add_u32 s46, s44, 262144\n\t"
      "s_addc_u32 s47, s45, 0\n\t"
      "s_add_u32 m0, s50, 32768\n\t"
      "s_waitcnt lgkmcnt(4)\n\t"
      "v_mfma_f32_32x32x16_bf16 v[18:33], v[38:41], v[138:141], v[18:33]\n\t"
      "s_waitcnt vmcnt(10)\n\t"
      "s_barrier\n\t"
      "ds_read_b64_tr_b16 v[34:35], v162 offset:49152\n\t"
      "ds_read_b64_tr_b16 v[36:37], v162 offset:50176\n\t"
      "global_load_lds_dwordx4 v164, s[46:47]\n\t"
      "s_add_u32 m0, s50, 33792\n\t"
      "s_waitcnt lgkmcnt(4)\n\t"
      "v_mfma_f32_32x32x16_bf16 v[2:17], v[42:45], v[142:145], v[2:17]\n\t"
      "ds_read_b64_tr_b16 v[38:39], v163 offset:49152\n\t"
      "ds_read_b64_tr_b16 v[40:41], v163 offset:50176\n\t"
      "global_load_lds_dwordx4 v165, s[46:47]\n\t"
      "s_waitcnt lgkmcnt(4)\n\t"
      "v_mfma_f32_32x32x16_bf16 v[18:33], v[46:49], v[142:145], v[18:33]\n\t"
      "ds_read_b64_tr_b16 v[42:43], v162 offset:51200\n\t"
      "ds_read_b64_tr_b16 v[44:45], v162 offset:52224\n\t"
      "s_waitcnt lgkmcnt(4)\n\t"
      "v_mfma_f32_32x32x16_bf16 v[2:17], v[34:37], v[146:149], v[2:17]\n\t"
      "ds_read_b64_tr_b16 v[46:47], v163 offset:51200\n\t"
      "ds_read_b64_tr_b16 v[48:49], v163 offset:52224\n\t"
      "s_waitcnt lgkmcnt(4)\n\t"
      "v_mfma_f32_32x32x16_bf16 v[18:33], v[38:41], v[146:149], v[18:33]\n\t"
      "ds_read_b64_tr_b16 v[34:35], v162 offset:53248\n\t"
      "ds_read_b64_tr_b16 v[36:37], v162 offset:54272\n\t"
      "s_waitcnt lgkmcnt(4)\n\t"
      "v_mfma_f32_32x32x16_bf16 v[2:17], v[42:45], v[150:153], v[2:17]\n\t"
      "ds_read_b64_tr_b16 v[38:39], v163 offset:53248\n\t"
      "ds_read_b64_tr_b16 v[40:41], v163 offset:54272\n\t"
      "s_waitcnt lgkmcnt(4)\n\t"
      "v_mfma_f32_32x32x16_bf16 v[18:33], v[46:49], v[150:153], v[18:33]\n\t"
      "ds_read_b64_tr_b16 v[42:43], v162 offset:55296\n\t"
      "ds_read_b64_tr_b16 v[44:45], v162 offset:56320\n\t"
      "s_waitcnt lgkmcnt(4)\n\t"
      "v_mfma_f32_32x32x16_bf16 v[2:17], v[34:37], v[154:157], v[2:17]\n\t"
      "ds_read_b64_tr_b16 v[46:47], v163 offset:55296\n\t"
      "ds_read_b64_tr_b16 v[48:49], v163 offset:56320\n\t"
      "s_waitcnt lgkmcnt(4)\n\t"
      "v_mfma_f32_32x32x16_bf16 v[18:33], v[38:41], v[154:157], v[18:33]\n\t"
      "s_waitcnt lgkmcnt(2)\n\t"
      "v_mfma_f32_32x32x16_bf16 v[2:17], v[42:45], v[158:161], v[2:17]\n\t"
      "s_waitcnt lgkmcnt(0)\n\t"
      "v_mfma_f32_32x32x16_bf16 v[18:33], v[46:49], v[158:161], v[18:33]\n\t"
      "s_mov_b32 m0, s59\n\t"
      "s_nop 7\n\t"
      "s_nop 7\n\t"
      : "+{v[2:17]}"(c0), "+{v[18:33]}"(c1), "+{s[40:41]}"(wsrc), "+{s[48:49]}"(hsrc)
      : "{v162}"(a0), "{v163}"(a1), "{v164}"(vw0), "{v165}"(vw1), "{v166}"(vb), "{s50}"(ldsw), "{s[44:45]}"(wosrc)
      : "v34", "v35", "v36", "v37", "v38", "v39", "v40", "v41", "v42", "v43", "v44", "v45", "v46", "v47", "v48", "v49", "v50", "v51", "v52", "v53", "v54", "v55", "v56", "v57", "v58", "v59", "v60", "v61", "v62", "v63", "v64", "v65", "v66", "v67", "v68", "v69", "v70", "v71", "v72", "v73", "v74", "v75", "v76", "v77", "v78", "v79", "v80", "v81", "v82", "v83", "v84", "v85", "v86", "v87", "v88", "v89", "v90", "v91", "v92", "v93", "v94", "v95", "v96", "v97", "v98", "v99", "v100", "v101", "v102", "v103", "v104", "v105", "v106", "v107", "v108", "v109", "v110", "v111", "v112", "v113", "v114", "v115", "v116", "v117", "v118", "v119", "v120", "v121", "v122", "v123", "v124", "v125", "v126", "v127", "v128", "v129", "v130", "v131", "v132", "v133", "v134", "v135", "v136", "v137", "v138", "v139", "v140", "v141", "v142", "v143", "v144", "v145", "v146", "v147", "v148", "v149", "v150", "v151", "v152", "v153", "v154", "v155", "v156", "v157", "v158", "v159", "v160", "v161", "s46", "s47", "s59", "scc", "memory");
  __builtin_amdgcn_s_setprio(PRIO_EPI);
  const bool keeper = (w >> 1) == fh;
  const int il = w & 1, kh = keeper ? 0 : 1;
  const __amdgpu_buffer_rsrc_t xr = __builtin_amdgcn_make_buffer_rsrc(ws + WS_DP, 0, 256 * 16384, 0x00020000);
  const int tt = 4 * tb + 2 * fh + il, ct = 2 * cb + kh, tok = tt * 32 + (lane & 31);
  u32x2 xv[4];
#pragma unroll
  for (int gq = 0; gq < 4; ++gq) xv[gq] = *(const u32x2*)(x + (size_t)tok * D_ + ct * 32 + 8 * gq + 4 * hh);
  if (!keeper) {
    const unsigned o0 = dg * 16384 + il * 8192 + lane * 16;
#pragma unroll
    for (int g4 = 0; g4 < 4; ++g4) {
      __builtin_amdgcn_raw_buffer_store_b128((u32x4){__float_as_uint(c0[4 * g4]), __float_as_uint(c0[4 * g4 + 1]), __float_as_uint(c0[4 * g4 + 2]), __float_as_uint(c0[4 * g4 + 3])}, xr, o0 + g4 * 1024, 0, 16);
      __builtin_amdgcn_raw_buffer_store_b128((u32x4){__float_as_uint(c1[4 * g4]), __float_as_uint(c1[4 * g4 + 1]), __float_as_uint(c1[4 * g4 + 2]), __float_as_uint(c1[4 * g4 + 3])}, xr, o0 + 4096 + g4 * 1024, 0, 16);
    }
  }
  LAS char* eb = (LAS char*)lds + 65536 + il * 8192 + lane * 16;
  auto put = [&](int slot, const f32x16& v) {
#pragma unroll
    for (int g4 = 0; g4 < 4; ++g4) *(LAS f32x4*)(eb + slot * 4096 + g4 * 1024) = (f32x4){v[4 * g4], v[4 * g4 + 1], v[4 * g4 + 2], v[4 * g4 + 3]};
  };
  if (keeper) put(0, c1);
  else {
    asm volatile("s_waitcnt vmcnt(0)" ::: "memory");
    if (lane == 0) set_word(ws, WS_PFL + (dg * 2 + il) * 4, tag);
  }
  BAR();
  glds16(wosrc + 5 * 65536, vw0, ldsw + 5 * 8192); glds16(wosrc + 5 * 65536, vw1, ldsw + 5 * 8192 + 1024);
  glds16(wosrc + 6 * 65536, vw0, ldsw + 6 * 8192); glds16(wosrc + 6 * 65536, vw1, ldsw + 6 * 8192 + 1024);
  f32x16 dn = c0;
  if (!keeper) {
#pragma unroll
    for (int g4 = 0; g4 < 4; ++g4) { const f32x4 t4 = *(const LAS f32x4*)(eb + g4 * 1024); dn[4 * g4] = t4[0]; dn[4 * g4 + 1] = t4[1]; dn[4 * g4 + 2] = t4[2]; dn[4 * g4 + 3] = t4[3]; }
  }
  wait_words(ws, WS_PFL + ((dg ^ 1) * 2 + il) * 4, 1, tag, lane);
  u32x4 pp4[4];
#pragma unroll
  for (int g4 = 0; g4 < 4; ++g4) pp4[g4] = __builtin_amdgcn_raw_buffer_load_b128(xr, (dg ^ 1) * 16384 + il * 8192 + kh * 4096 + g4 * 1024 + lane * 16, 0, 16);
  float gv[16], xf[16];
#pragma unroll
  for (int r = 0; r < 16; ++r) {
    const float d = dn[r] + __uint_as_float(pp4[r >> 2][r & 3]);
    gv[r] = d * __builtin_amdgcn_rcpf(1.0f + __builtin_amdgcn_exp2f(-1.4426950408889634f * d));
    const unsigned xu = xv[r >> 2][(r & 3) >> 1];
    xf[r] = (r & 1) ? bf_hi(xu) : bf_lo(xu);
  }
  asm volatile("" : "+v"(gv[0]), "+v"(gv[1]), "+v"(gv[2]), "+v"(gv[3]), "+v"(gv[4]), "+v"(gv[5]), "+v"(gv[6]), "+v"(gv[7]), "+v"(gv[8]), "+v"(gv[9]), "+v"(gv[10]), "+v"(gv[11]),
               "+v"(gv[12]), "+v"(gv[13]), "+v"(gv[14]), "+v"(gv[15]));
  WAIT_BAR(0);
  wait_words(ws, WS_OFL + (tt * 16 + 8 * kh) * 4, 8, tag, lane);
  u32x4 of[16];
  {
    const char* ob = unip(ws + WS_OF + ((size_t)tt * 32 + 16 * kh) * 1024);
    const unsigned vo = lane * 16, vo2 = vo + 4096, vo3 = vo + 8192, vo4 = vo + 12288;
    asm volatile(
        "global_load_dwordx4 %0, %16, %20 sc1\n\tglobal_load_dwordx4 %1, %16, %20 offset:1024 sc1\n\tglobal_load_dwordx4 %2, %16, %20 offset:2048 sc1\n\tglobal_load_dwordx4 %3, %16, %20 offset:3072 sc1\n\t"
        "global_load_dwordx4 %4, %17, %20 sc1\n\tglobal_load_dwordx4 %5, %17, %20 offset:1024 sc1\n\tglobal_load_dwordx4 %6, %17, %20 offset:2048 sc1\n\tglobal_load_dwordx4 %7, %17, %20 offset:3072 sc1\n\t"
        "global_load_dwordx4 %8, %18, %20 sc1\n\tglobal_load_dwordx4 %9, %18, %20 offset:1024 sc1\n\tglobal_load_dwordx4 %10, %18, %20 offset:2048 sc1\n\tglobal_load_dwordx4 %11, %18, %20 offset:3072 sc1\n\t"
        "global_load_dwordx4 %12, %19, %20 sc1\n\tglobal_load_dwordx4 %13, %19, %20 offset:1024 sc1\n\tglobal_load_dwordx4 %14, %19, %20 offset:2048 sc1\n\tglobal_load_dwordx4 %15, %19, %20 offset:3072 sc1"
        : "=&v"(of[0]), "=&v"(of[1]), "=&v"(of[2]), "=&v"(of[3]), "=&v"(of[4]), "=&v"(of[5]), "=&v"(of[6]), "=&v"(of[7]),
          "=&v"(of[8]), "=&v"(of[9]), "=&v"(of[10]), "=&v"(of[11]), "=&v"(of[12]), "=&v"(of[13]), "=&v"(of[14]), "=&v"(of[15])
        : "v"(vo), "v"(vo2), "v"(vo3), "v"(vo4), "s"(ob) : "memory");
  }
  f32x16 s0 = {}, s1 = {};
  {
    lds_cptr ap0 = (lds_cptr)lds + (a0 - lb) + kh * 16 * 2048, ap1 = (lds_cptr)lds + (a1 - lb) + kh * 16 * 2048;
#define FIN_STEP(i, n) { const bf16x8 f0 = trfrag(ap0 + (i) * 2048, 1024), f1 = trfrag(ap1 + (i) * 2048, 1024); \
      asm volatile("s_waitcnt vmcnt(" #n ")" : "+v"(of[i]) :: "memory"); __builtin_amdgcn_sched_barrier(0); \
      const bf16x8 bf = __builtin_bit_cast(bf16x8, of[i]); s0 = MFMA32(f0, bf, s0); s1 = MFMA32(f1, bf, s1); }
    FIN_STEP(0, 15) FIN_STEP(1, 14) FIN_STEP(2, 13) FIN_STEP(3, 12) FIN_STEP(4, 11) FIN_STEP(5, 10) FIN_STEP(6, 9) FIN_STEP(7, 8)
    FIN_STEP(8, 7) FIN_STEP(9, 6) FIN_STEP(10, 5) FIN_STEP(11, 4) FIN_STEP(12, 3) FIN_STEP(13, 2) FIN_STEP(14, 1) FIN_STEP(15, 0)
#undef FIN_STEP
  }
  put(keeper ? 1 : 0, keeper ? s1 : s0);
  BAR();
  {
    const f32x16 sa = keeper ? s0 : s1;
    const int row = 32 * il + (lane & 31);
#pragma unroll
    for (int g4 = 0; g4 < 4; ++g4) {
      const f32x4 a4 = *(const LAS f32x4*)(eb + (keeper ? 0 : 1) * 4096 + g4 * 1024);
      float r[4];
#pragma unroll
      for (int e = 0; e < 4; ++e) r[e] = xf[4 * g4 + e] + (sa[4 * g4 + e] + a4[e]) * gv[4 * g4 + e];
      *(LAS u32x2*)((LAS char*)lds + row * 128 + (((kh * 4 + g4) ^ (row & 7)) * 16) + hh * 8) = (u32x2){cvtpk(r[0], r[1]), cvtpk(r[2], r[3])};
    }
  }
  BAR();
  {
#pragma unroll
    for (int i = 0; i < 2; ++i) {
      const int row = 16 * w + 8 * i + (lane >> 3), un = lane & 7;
      const u32x4 v = *(const LAS u32x4*)((lds_cptr)lds + row * 128 + ((un ^ (row & 7)) * 16));
      *(u32x4*)(out + (size_t)((4 * tb + 2 * fh) * 32 + row) * D_ + cb * 64 + un * 8) = v;
    }
  }
}

__device__ __forceinline__ void map_attn(int b, int& ip, int& c) { ip = (b & 7) * 4 + ((b >> 3) & 3); c = b >> 5; }
__device__ __forceinline__ void map_ffn(int b, int& tb, int& u2, int& fh) { const int xg = b & 7, u = b >> 3; tb = 4 * (xg >> 1) + (u & 3); u2 = u >> 2; fh = xg & 1; }


constexpr int K_LDS = 81920;
static_assert(A_LDS + 256 <= K_LDS && F_LDS + 256 <= K_LDS && KV_LDS <= K_LDS, "LDS budget");
__global__ __launch_bounds__(256, 2) void k_block(const u16* __restrict__ x, const u16* __restrict__ wq, const u16* __restrict__ wk, const u16* __restrict__ wv, const u16* __restrict__ wo,
                                                  const u16* __restrict__ wg, const u16* __restrict__ wu, const u16* __restrict__ wd, u16* __restrict__ out, char* __restrict__ ws) {
  extern __shared__ __attribute__((aligned(16))) char lds[];
  const int b = blockIdx.x & 255;
  const unsigned genv = __hip_atomic_load((gu32*)(ws + WS_GEN), __ATOMIC_RELAXED, __HIP_MEMORY_SCOPE_AGENT);
  if (blockIdx.x < 256) {
    __builtin_amdgcn_s_setprio(PRIO_EPI);
    const unsigned tag = kv_prep(b, genv, x, wk, wv, ws, lds);
    __builtin_amdgcn_s_setprio(PRIO_FFN);
    int tb, u2, fh; map_ffn(b, tb, u2, fh);
    gateup_role(tb, 8 * fh + u2, WS_HFL + ((tb * 2 + fh) * 32 + u2) * 4, tag, wg, wu, ws, lds);
    down_role(tb, u2, fh, b, tag, wd, wo, x, out, ws, lds);
    if (b == 0 && threadIdx.x < 64) {
#pragma unroll 1
      for (int i = 0; i < 4; ++i) wait_all256(ws, WS_OFL + i * 1024, tag, threadIdx.x);
      if (threadIdx.x == 0) set_word(ws, WS_GEN, tag);
    }
  } else {
    __builtin_amdgcn_s_setprio(PRIO_ATTN);
    if (__builtin_amdgcn_readfirstlane(threadIdx.x >> 6) == 3) {
      const int l = threadIdx.x & 63;
      const char* p = ws + (size_t)(l < 34 ? l : 0) * 524288;
      if (l >= 34 && l < 38) p = (const char*)out + (l - 34) * 524288;
      if (l >= 38 && l < 42) p = (const char*)wg + (l - 38) * 524288;
      if (l >= 42 && l < 46) p = (const char*)wu + (l - 42) * 524288;
      if (l >= 46 && l < 50) p = (const char*)wd + (l - 46) * 524288;
      if (l == 50) p = (const char*)wo;
      unsigned sv;
      asm volatile("s_mov_b32 %0, m0\n\ts_mov_b32 m0, %2\n\ts_nop 0\n\tglobal_load_lds_dword %1, off\n\ts_mov_b32 m0, %0" : "=&s"(sv) : "v"(p), "s"(uni((unsigned)(uintptr_t)lds + A_LDS)) : "memory");
    }
    int ip, c; map_attn(b, ip, c);
    attn_role(ip, c, b, genv, x, wq, ws, lds);
  }
}
extern "C" void kernel_launch(void* const* d_in, const int* in_sizes, int n_in, void* d_out, int out_size, void* d_ws, size_t ws_size, hipStream_t stream) {
  (void)hipFuncSetAttribute((const void*)k_block, hipFuncAttributeMaxDynamicSharedMemorySize, K_LDS);
  k_block<<<512, 256, K_LDS, stream>>>((const u16*)d_in[0], (const u16*)d_in[1], (const u16*)d_in[2], (const u16*)d_in[3], (const u16*)d_in[4], (const u16*)d_in[6],
                                       (const u16*)d_in[7], (const u16*)d_in[8], (u16*)d_out, (char*)d_ws);
}
```
